```python
import torch

def _softmax(x, axis=-1):
    return torch.softmax(x, dim=axis)
def _gelu(x):
    return torch.nn.functional.gelu(x, approximate="tanh")
def _silu(x):
    return torch.nn.functional.silu(x)
def _sigmoid(x):
    return torch.sigmoid(x)
def _layernorm(x, axis=-1, eps=1e-5):
    m = x.mean(dim=axis, keepdim=True)
    v = x.var(dim=axis, keepdim=True, unbiased=False)
    return (x - m) / torch.sqrt(v + eps)
def _scatter_add(tgt, idx, src, axis=0):
    return tgt.clone().index_add_(axis, idx, src)
def _segment_sum(src, seg, n):
    out = torch.zeros((n,) + src.shape[1:], dtype=src.dtype)
    return out.index_add_(0, seg, src)
def _pad(x, pw):
    flat = tuple(v for pair in reversed(pw) for v in pair)
    return torch.nn.functional.pad(x, flat)

kernel_name = 'interleave_layers__spectral_fnet__moe_arch_mod__prompt'

def setup_inputs():

    _w = (lambda a: (lambda t: t.to(torch.bfloat16) if t.is_floating_point() else t)(torch.as_tensor(a)))
    def _A_setup():
        B, S, D = 1, 2048, 512
        ns = torch.arange(S)[:, None] * torch.arange(S)[None, :]
        nd = torch.arange(D)[:, None] * torch.arange(D)[None, :]
        Ws_re = torch.cos(-2.0 * torch.pi * ns / S).to(float)
        Ws_im = torch.sin(-2.0 * torch.pi * ns / S).to(float)
        Wd_re = torch.cos(-2.0 * torch.pi * nd / D).to(float)
        Wd_im = torch.sin(-2.0 * torch.pi * nd / D).to(float)
        return {
            "x": _w((lambda *s: torch.randn(*s, dtype=torch.bfloat16) * 0.05)(B, S, D)),
            "dft_seq_re": _w(Ws_re),
            "dft_seq_im": _w(Ws_im),
            "dft_hid_re": _w(Wd_re),
            "dft_hid_im": _w(Wd_im),
        }
    _A_in = _A_setup()

    def _B_setup():
        B, S, D, F, C = 1, 2048, 512, 1792, 2
        return {
            "x": _w((lambda *s: torch.randn(*s, dtype=torch.bfloat16) * 0.05)(B, S, D)),
            "w_r": _w((lambda *s: torch.randn(*s, dtype=torch.bfloat16) * 1.6)(D)),
            "w_gate": _w((lambda *s: torch.randn(*s, dtype=torch.bfloat16) * 0.4)(D, F)),
            "w_up": _w((lambda *s: torch.randn(*s, dtype=torch.bfloat16) * 0.4)(D, F)),
            "w_down": _w((lambda *s: torch.randn(*s, dtype=torch.bfloat16) * 0.4)(F, D)),
        }
    _B_in = _B_setup()

    return {
        "A_x": _A_in["x"],
        "A_dft_seq_re": _A_in["dft_seq_re"],
        "A_dft_seq_im": _A_in["dft_seq_im"],
        "A_dft_hid_re": _A_in["dft_hid_re"],
        "A_dft_hid_im": _A_in["dft_hid_im"],
        "B_x": _B_in["x"],
        "B_w_r": _B_in["w_r"],
        "B_w_gate": _B_in["w_gate"],
        "B_w_up": _B_in["w_up"],
        "B_w_down": _B_in["w_down"],
    }

def reference(A_x, A_dft_seq_re, A_dft_seq_im, A_dft_hid_re, A_dft_hid_im, B_x, B_w_r, B_w_gate, B_w_up, B_w_down):
    def _A_ref(x, dft_seq_re, dft_seq_im, dft_hid_re, dft_hid_im):
        a = torch.einsum("bsd,ed->bse", x, dft_hid_re)
        b = torch.einsum("bsd,ed->bse", x, dft_hid_im)
        y = torch.einsum("ts,bse->bte", dft_seq_re, a) - torch.einsum("ts,bse->bte", dft_seq_im, b)
        return y

    def _B_ref(x, w_r, w_gate, w_up, w_down):
        B, S, D = x.shape
        C = 2
        r = x @ w_r
        idx = torch.argsort(-r, axis=-1)[:, :C]
        idx_e = (lambda x, n, axis: torch.repeat_interleave(x, n, dim=axis))(idx.reshape(B, C, 1), D, axis=2)
        x_sel = (lambda x, i, axis: torch.take_along_dim(x, i, dim=axis))(x, idx_e, axis=1)
        g = _silu(x_sel @ w_gate)
        u = x_sel @ w_up
        y = (g * u) @ w_down
        r_sel = _sigmoid((lambda x, i, axis: torch.take_along_dim(x, i, dim=axis))(r, idx, axis=1))
        y = y * r_sel.reshape(B, C, 1)
        b_off = (torch.arange(B) * S).reshape(B, 1)
        flat_idx = (idx + b_off).reshape(B * C)
        zero = (x * 0.0).reshape(B * S, D)
        upd = _scatter_add(zero, flat_idx, y.reshape(B * C, D), axis=0)
        return x + upd.reshape(B, S, D)

    x = A_x
    x = x + _A_ref(_layernorm(x, axis=-1), A_dft_seq_re, A_dft_seq_im, A_dft_hid_re, A_dft_hid_im)
    x = x + _B_ref(_layernorm(x, axis=-1), B_w_r, B_w_gate, B_w_up, B_w_down)
    x = x + _A_ref(_layernorm(x, axis=-1), A_dft_seq_re, A_dft_seq_im, A_dft_hid_re, A_dft_hid_im)
    x = x + _B_ref(_layernorm(x, axis=-1), B_w_r, B_w_gate, B_w_up, B_w_down)
    return x
```

```cpp
#include <hip/hip_runtime.h>
#include <hip/hip_bf16.h>
#include <stdint.h>

typedef unsigned short u16;
typedef __attribute__((ext_vector_type(8))) short bf16x8;
typedef __attribute__((ext_vector_type(4))) float f32x4;
typedef __attribute__((ext_vector_type(16))) float f32x16;

constexpr int S = 2048, D = 512, F = 1792;

__device__ __forceinline__ float bf2f(u16 b) { return __builtin_bit_cast(float, (unsigned)b << 16); }
__device__ __forceinline__ u16 f2bf(float f) { __hip_bfloat16 h = __float2bfloat16(f); return __builtin_bit_cast(u16, h); }
__device__ __forceinline__ float rbf(float f) { return bf2f(f2bf(f)); }

__device__ __forceinline__ float wave_sum(float v) {
  for (int o = 32; o > 0; o >>= 1) v += __shfl_xor(v, o);
  return v;
}

__device__ __forceinline__ void ln_row(const float (&x)[8], float (&y)[8]) {
  float s = 0; for (int i = 0; i < 8; ++i) s += x[i];
  s = wave_sum(s);
  float m = s * (1.f / D);
  float q = 0; for (int i = 0; i < 8; ++i) { float d = x[i] - m; q += d * d; }
  q = wave_sum(q);
  float v = rbf(q * (1.f / D));
  float mb = rbf(m);
  float sd = rbf(sqrtf(rbf(v + 1e-5f)));
  for (int i = 0; i < 8; ++i) y[i] = rbf(rbf(x[i] - mb) / sd);
}

__global__ void k_ln(const u16* x, u16* xn) {
  int row = blockIdx.x * 4 + (threadIdx.x >> 6), lane = threadIdx.x & 63;
  float v[8], y[8];
  bf16x8 a = *(const bf16x8*)(x + row * D + lane * 8);
  for (int i = 0; i < 8; ++i) v[i] = bf2f((u16)a[i]);
  ln_row(v, y);
  bf16x8 o; for (int i = 0; i < 8; ++i) o[i] = (short)f2bf(y[i]);
  *(bf16x8*)(xn + row * D + lane * 8) = o;
}

__global__ void k_gemm_nt(const u16* A, const u16* B, u16* C, int K, int lda, int ldb, int ldc) {
  int lane = threadIdx.x & 63, w = threadIdx.x >> 6;
  int m0 = blockIdx.y * 64 + (w >> 1) * 32, n0 = blockIdx.x * 64 + (w & 1) * 32;
  f32x16 acc = {};
  const u16* ap = A + (size_t)(m0 + (lane & 31)) * lda + (lane >> 5) * 8;
  const u16* bp = B + (size_t)(n0 + (lane & 31)) * ldb + (lane >> 5) * 8;
  for (int k = 0; k < K; k += 16) {
    bf16x8 a = *(const bf16x8*)(ap + k), b = *(const bf16x8*)(bp + k);
    acc = __builtin_amdgcn_mfma_f32_32x32x16_bf16(a, b, acc, 0, 0, 0);
  }
  for (int r = 0; r < 16; ++r) {
    int row = (r & 3) + 8 * (r >> 2) + 4 * (lane >> 5);
    C[(size_t)(m0 + row) * ldc + n0 + (lane & 31)] = f2bf(acc[r]);
  }
}

__global__ void k_post(const u16* x, const u16* y1, const u16* y2, const u16* wr, u16* x1o, u16* xno, float* r) {
  int row = blockIdx.x * 4 + (threadIdx.x >> 6), lane = threadIdx.x & 63;
  float v[8], y[8];
  bf16x8 a = *(const bf16x8*)(x + row * D + lane * 8);
  bf16x8 b = *(const bf16x8*)(y1 + row * D + lane * 8);
  bf16x8 c = *(const bf16x8*)(y2 + row * D + lane * 8);
  bf16x8 w = *(const bf16x8*)(wr + lane * 8);
  for (int i = 0; i < 8; ++i) v[i] = rbf(bf2f((u16)a[i]) + rbf(bf2f((u16)b[i]) - bf2f((u16)c[i])));
  ln_row(v, y);
  float d = 0; for (int i = 0; i < 8; ++i) d += y[i] * bf2f((u16)w[i]);
  d = wave_sum(d);
  if (lane == 0) r[row] = rbf(d);
  bf16x8 o, o1; for (int i = 0; i < 8; ++i) { o[i] = (short)f2bf(y[i]); o1[i] = (short)f2bf(v[i]); }
  *(bf16x8*)(xno + row * D + lane * 8) = o;
  *(bf16x8*)(x1o + row * D + lane * 8) = o1;
}

__device__ void top2(const float* r, int& i0, int& i1, float* sv, int* si) {
  int t = threadIdx.x;
  float b0 = -INFINITY, b1 = -INFINITY; int j0 = 0x7fffffff, j1 = 0x7fffffff;
  for (int i = t; i < S; i += 256) { float v = r[i]; if (v > b0) { b1 = b0; j1 = j0; b0 = v; j0 = i; } else if (v > b1) { b1 = v; j1 = i; } }
  sv[t * 2] = b0; sv[t * 2 + 1] = b1; si[t * 2] = j0; si[t * 2 + 1] = j1;
  __syncthreads();
  b0 = -INFINITY; b1 = -INFINITY; j0 = 0x7fffffff; j1 = 0x7fffffff;
  for (int i = 0; i < 512; ++i) { float v = sv[i]; int j = si[i];
    if (v > b0 || (v == b0 && j < j0)) { b1 = b0; j1 = j0; b0 = v; j0 = j; } else if (v > b1 || (v == b1 && j < j1)) { b1 = v; j1 = j; } }
  i0 = j0; i1 = j1;
}

__global__ void k_gateup(const float* r, const u16* xn, const u16* wg, const u16* wu, float* h, int* idx) {
  __shared__ float sv[512]; __shared__ int si[512];
  int i0, i1; top2(r, i0, i1, sv, si);
  if (blockIdx.x == 0 && threadIdx.x == 0) { idx[0] = i0; idx[1] = i1; }
  int f = blockIdx.x * 256 + threadIdx.x;
  float g0 = 0, g1 = 0, u0 = 0, u1 = 0;
  for (int d = 0; d < D; ++d) {
    float a = bf2f(xn[i0 * D + d]), b = bf2f(xn[i1 * D + d]);
    float G = bf2f(wg[d * F + f]), U = bf2f(wu[d * F + f]);
    g0 += a * G; g1 += b * G; u0 += a * U; u1 += b * U;
  }
  g0 = rbf(g0); g1 = rbf(g1); u0 = rbf(u0); u1 = rbf(u1);
  g0 = rbf(g0 / (1.f + __expf(-g0))); g1 = rbf(g1 / (1.f + __expf(-g1)));
  h[f] = rbf(g0 * u0); h[F + f] = rbf(g1 * u1);
}

__global__ void k_down(const float* h, const u16* wd, const float* r, const int* idx, float* upd) {
  int d = blockIdx.x * 256 + threadIdx.x;
  float y0 = 0, y1 = 0;
  for (int f = 0; f < F; ++f) { float w = bf2f(wd[f * D + d]); y0 += h[f] * w; y1 += h[F + f] * w; }
  float s0 = rbf(1.f / (1.f + __expf(-r[idx[0]]))), s1 = rbf(1.f / (1.f + __expf(-r[idx[1]])));
  upd[d] = rbf(rbf(y0) * s0); upd[D + d] = rbf(rbf(y1) * s1);
}

__global__ void k_fin(const u16* x1, const u16* xn, const float* upd, const int* idx, u16* x2) {
  int i = blockIdx.x * 256 + threadIdx.x;
  int row = i / D, d = i % D;
  float u = row == idx[0] ? upd[d] : (row == idx[1] ? upd[D + d] : 0.f);
  x2[i] = f2bf(bf2f(x1[i]) + rbf(bf2f(xn[i]) + u));
}

extern "C" void kernel_launch(void* const* d_in, const int* in_sizes, int n_in, void* d_out, int out_size, void* d_ws, size_t ws_size, hipStream_t stream) {
  const u16* x = (const u16*)d_in[0]; const u16* Cr = (const u16*)d_in[1]; const u16* Ci = (const u16*)d_in[2];
  const u16* Dr = (const u16*)d_in[3]; const u16* Di = (const u16*)d_in[4];
  const u16* wr = (const u16*)d_in[6]; const u16* wg = (const u16*)d_in[7]; const u16* wu = (const u16*)d_in[8]; const u16* wd = (const u16*)d_in[9];
  char* ws = (char*)d_ws; const size_t MB = 1 << 20;
  u16* xn = (u16*)(ws); u16* aT = (u16*)(ws + 2 * MB); u16* bT = (u16*)(ws + 4 * MB);
  u16* y1 = (u16*)(ws + 6 * MB); u16* y2 = (u16*)(ws + 8 * MB); u16* x1 = (u16*)(ws + 10 * MB); u16* x2 = (u16*)(ws + 12 * MB);
  float* r = (float*)(ws + 14 * MB); float* h = (float*)(ws + 15 * MB); float* upd = (float*)(ws + 16 * MB); int* idx = (int*)(ws + 17 * MB);
  const u16* cur = x;
  for (int l = 0; l < 2; ++l) {
    k_ln<<<S / 4, 256, 0, stream>>>(cur, xn);
    k_gemm_nt<<<dim3(S / 64, D / 64), 256, 0, stream>>>(Dr, xn, aT, D, D, D, S);
    k_gemm_nt<<<dim3(S / 64, D / 64), 256, 0, stream>>>(Di, xn, bT, D, D, D, S);
    k_gemm_nt<<<dim3(D / 64, S / 64), 256, 0, stream>>>(Cr, aT, y1, S, S, S, D);
    k_gemm_nt<<<dim3(D / 64, S / 64), 256, 0, stream>>>(Ci, bT, y2, S, S, S, D);
    k_post<<<S / 4, 256, 0, stream>>>(cur, y1, y2, wr, x1, xn, r);
    k_gateup<<<F / 256, 256, 0, stream>>>(r, xn, wg, wu, h, idx);
    k_down<<<D / 256, 256, 0, stream>>>(h, wd, r, idx, upd);
    u16* o = l == 0 ? x2 : (u16*)d_out;
    k_fin<<<S * D / 256, 256, 0, stream>>>(x1, xn, upd, idx, o);
    cur = o;
  }
}
```

```cpp
#include <hip/hip_runtime.h>
#include <hip/hip_bf16.h>
#include <stdint.h>

typedef unsigned short u16;
typedef __attribute__((ext_vector_type(8))) short bf16x8;
typedef __attribute__((ext_vector_type(4))) float f32x4;
typedef __attribute__((ext_vector_type(16))) float f32x16;

constexpr int S = 2048, D = 512, F = 1792;

__device__ __forceinline__ float bf2f(u16 b) { return __builtin_bit_cast(float, (unsigned)b << 16); }
__device__ __forceinline__ u16 f2bf(float f) { __hip_bfloat16 h = __float2bfloat16(f); return __builtin_bit_cast(u16, h); }
__device__ __forceinline__ float rbf(float f) { return bf2f(f2bf(f)); }

__device__ __forceinline__ float wave_sum(float v) {
  for (int o = 32; o > 0; o >>= 1) v += __shfl_xor(v, o);
  return v;
}

__device__ __forceinline__ void ln_row(const float (&x)[8], float (&y)[8]) {
  float s = 0; for (int i = 0; i < 8; ++i) s += x[i];
  s = wave_sum(s);
  float m = s * (1.f / D);
  float q = 0; for (int i = 0; i < 8; ++i) { float d = x[i] - m; q += d * d; }
  q = wave_sum(q);
  float v = rbf(q * (1.f / D));
  float mb = rbf(m);
  float sd = rbf(sqrtf(rbf(v + 1e-5f)));
  for (int i = 0; i < 8; ++i) y[i] = rbf(rbf(x[i] - mb) / sd);
}

__global__ void k_ln(const u16* x, u16* xn) {
  int row = blockIdx.x * 4 + (threadIdx.x >> 6), lane = threadIdx.x & 63;
  float v[8], y[8];
  bf16x8 a = *(const bf16x8*)(x + row * D + lane * 8);
  for (int i = 0; i < 8; ++i) v[i] = bf2f((u16)a[i]);
  ln_row(v, y);
  bf16x8 o; for (int i = 0; i < 8; ++i) o[i] = (short)f2bf(y[i]);
  *(bf16x8*)(xn + row * D + lane * 8) = o;
}

__global__ void k_gemm_nt(const u16* A, const u16* B, u16* C, int K, int lda, int ldb, int ldc) {
  int lane = threadIdx.x & 63, w = threadIdx.x >> 6;
  int m0 = blockIdx.y * 64 + (w >> 1) * 32, n0 = blockIdx.x * 64 + (w & 1) * 32;
  f32x16 acc = {};
  const u16* ap = A + (size_t)(m0 + (lane & 31)) * lda + (lane >> 5) * 8;
  const u16* bp = B + (size_t)(n0 + (lane & 31)) * ldb + (lane >> 5) * 8;
  for (int k = 0; k < K; k += 16) {
    bf16x8 a = *(const bf16x8*)(ap + k), b = *(const bf16x8*)(bp + k);
    acc = __builtin_amdgcn_mfma_f32_32x32x16_bf16(a, b, acc, 0, 0, 0);
  }
  for (int r = 0; r < 16; ++r) {
    int row = (r & 3) + 8 * (r >> 2) + 4 * (lane >> 5);
    C[(size_t)(m0 + row) * ldc + n0 + (lane & 31)] = f2bf(acc[r]);
  }
}

__global__ void k_post(const u16* x, const u16* y1, const u16* y2, const u16* wr, u16* x1o, u16* xno, float* r) {
  int row = blockIdx.x * 4 + (threadIdx.x >> 6), lane = threadIdx.x & 63;
  float v[8], y[8];
  bf16x8 a = *(const bf16x8*)(x + row * D + lane * 8);
  bf16x8 b = *(const bf16x8*)(y1 + row * D + lane * 8);
  bf16x8 c = *(const bf16x8*)(y2 + row * D + lane * 8);
  bf16x8 w = *(const bf16x8*)(wr + lane * 8);
  for (int i = 0; i < 8; ++i) v[i] = rbf(bf2f((u16)a[i]) + rbf(bf2f((u16)b[i]) - bf2f((u16)c[i])));
  ln_row(v, y);
  float d = 0; for (int i = 0; i < 8; ++i) d += y[i] * bf2f((u16)w[i]);
  d = wave_sum(d);
  if (lane == 0) r[row] = rbf(d);
  bf16x8 o, o1; for (int i = 0; i < 8; ++i) { o[i] = (short)f2bf(y[i]); o1[i] = (short)f2bf(v[i]); }
  *(bf16x8*)(xno + row * D + lane * 8) = o;
  *(bf16x8*)(x1o + row * D + lane * 8) = o1;
}

__device__ void top2(const float* r, int& i0, int& i1, float* sv, int* si) {
  int t = threadIdx.x;
  float b0 = -INFINITY, b1 = -INFINITY; int j0 = 0x7fffffff, j1 = 0x7fffffff;
  for (int i = t; i < S; i += 256) { float v = r[i]; if (v > b0) { b1 = b0; j1 = j0; b0 = v; j0 = i; } else if (v > b1) { b1 = v; j1 = i; } }
  sv[t * 2] = b0; sv[t * 2 + 1] = b1; si[t * 2] = j0; si[t * 2 + 1] = j1;
  __syncthreads();
  b0 = -INFINITY; b1 = -INFINITY; j0 = 0x7fffffff; j1 = 0x7fffffff;
  for (int i = 0; i < 512; ++i) { float v = sv[i]; int j = si[i];
    if (v > b0 || (v == b0 && j < j0)) { b1 = b0; j1 = j0; b0 = v; j0 = j; } else if (v > b1 || (v == b1 && j < j1)) { b1 = v; j1 = j; } }
  i0 = j0; i1 = j1;
}

__global__ void k_gateup(const float* r, const u16* xn, const u16* wg, const u16* wu, float* h, int* idx) {
  __shared__ float sv[512]; __shared__ int si[512];
  int i0, i1; top2(r, i0, i1, sv, si);
  if (blockIdx.x == 0 && threadIdx.x == 0) { idx[0] = i0; idx[1] = i1; }
  int f = blockIdx.x * 256 + threadIdx.x;
  float g0 = 0, g1 = 0, u0 = 0, u1 = 0;
  for (int d = 0; d < D; ++d) {
    float a = bf2f(xn[i0 * D + d]), b = bf2f(xn[i1 * D + d]);
    float G = bf2f(wg[d * F + f]), U = bf2f(wu[d * F + f]);
    g0 += a * G; g1 += b * G; u0 += a * U; u1 += b * U;
  }
  g0 = rbf(g0); g1 = rbf(g1); u0 = rbf(u0); u1 = rbf(u1);
  g0 = rbf(g0 / (1.f + __expf(-g0))); g1 = rbf(g1 / (1.f + __expf(-g1)));
  h[f] = rbf(g0 * u0); h[F + f] = rbf(g1 * u1);
}

__global__ void k_down(const float* h, const u16* wd, const float* r, const int* idx, float* upd) {
  int d = blockIdx.x * 256 + threadIdx.x;
  float y0 = 0, y1 = 0;
  for (int f = 0; f < F; ++f) { float w = bf2f(wd[f * D + d]); y0 += h[f] * w; y1 += h[F + f] * w; }
  float s0 = rbf(1.f / (1.f + __expf(-r[idx[0]]))), s1 = rbf(1.f / (1.f + __expf(-r[idx[1]])));
  upd[d] = rbf(rbf(y0) * s0); upd[D + d] = rbf(rbf(y1) * s1);
}

__global__ void k_fin(const u16* x1, const u16* xn, const float* upd, const int* idx, u16* x2) {
  int i = blockIdx.x * 256 + threadIdx.x;
  int row = i / D, d = i % D;
  float u = row == idx[0] ? upd[d] : (row == idx[1] ? upd[D + d] : 0.f);
  x2[i] = f2bf(bf2f(x1[i]) + rbf(bf2f(xn[i]) + u));
}

#define PROBE 3
#define WSOFF (32u<<20)
#define PROBE_WG 100

typedef unsigned v4u __attribute__((ext_vector_type(4)));
typedef unsigned long long u64;
__device__ __forceinline__ u64 rt() { return __builtin_amdgcn_s_memrealtime(); }
#define LAS __attribute__((address_space(3)))
__device__ __forceinline__ void amplify(u64 t0, u64 t1) {
  if (blockIdx.x == PROBE_WG) { u64 end = t1 + (t1 - t0) * 100; while (rt() < end) __builtin_amdgcn_s_sleep(8); }
}
__global__ void __launch_bounds__(256, 1) k_probe(const char* cr, const char* ci, const char* ws) {
  int b = blockIdx.x, lane = threadIdx.x & 63, w = __builtin_amdgcn_readfirstlane(threadIdx.x >> 6);
  u64 t0 = rt();
  const __amdgpu_buffer_rsrc_t rs = __builtin_amdgcn_make_buffer_rsrc((void*)(ws + WSOFF), 0, 1 << 24, 0x00020000);
  unsigned a = 0;
  for (int i = 0; i < 8; ++i) { unsigned v = __builtin_amdgcn_raw_buffer_load_b32(rs, (b * 8 + i) * 4096 + a, 0, 16); asm volatile("v_and_b32 %0, 0, %1" : "=v"(a) : "v"(v)); }
  asm volatile("" :: "v"(a));
  __syncthreads();
  u64 t1 = rt();
  amplify(t0, t1);
}

extern "C" void kernel_launch(void* const* d_in, const int* in_sizes, int n_in, void* d_out, int out_size, void* d_ws, size_t ws_size, hipStream_t stream) {
  const u16* x = (const u16*)d_in[0]; const u16* Cr = (const u16*)d_in[1]; const u16* Ci = (const u16*)d_in[2];
  const u16* Dr = (const u16*)d_in[3]; const u16* Di = (const u16*)d_in[4];
  const u16* wr = (const u16*)d_in[6]; const u16* wg = (const u16*)d_in[7]; const u16* wu = (const u16*)d_in[8]; const u16* wd = (const u16*)d_in[9];
  char* ws = (char*)d_ws; const size_t MB = 1 << 20;
  u16* xn = (u16*)(ws); u16* aT = (u16*)(ws + 2 * MB); u16* bT = (u16*)(ws + 4 * MB);
  u16* y1 = (u16*)(ws + 6 * MB); u16* y2 = (u16*)(ws + 8 * MB); u16* x1 = (u16*)(ws + 10 * MB); u16* x2 = (u16*)(ws + 12 * MB);
  float* r = (float*)(ws + 14 * MB); float* h = (float*)(ws + 15 * MB); float* upd = (float*)(ws + 16 * MB); int* idx = (int*)(ws + 17 * MB);
  const u16* cur = x;
  k_probe<<<256, 256, 0, stream>>>((const char*)Cr, (const char*)Ci, (const char*)d_ws);
  for (int l = 0; l < 2; ++l) {
    k_ln<<<S / 4, 256, 0, stream>>>(cur, xn);
    k_gemm_nt<<<dim3(S / 64, D / 64), 256, 0, stream>>>(Dr, xn, aT, D, D, D, S);
    k_gemm_nt<<<dim3(S / 64, D / 64), 256, 0, stream>>>(Di, xn, bT, D, D, D, S);
    k_gemm_nt<<<dim3(D / 64, S / 64), 256, 0, stream>>>(Cr, aT, y1, S, S, S, D);
    k_gemm_nt<<<dim3(D / 64, S / 64), 256, 0, stream>>>(Ci, bT, y2, S, S, S, D);
    k_post<<<S / 4, 256, 0, stream>>>(cur, y1, y2, wr, x1, xn, r);
    k_gateup<<<F / 256, 256, 0, stream>>>(r, xn, wg, wu, h, idx);
    k_down<<<D / 256, 256, 0, stream>>>(h, wd, r, idx, upd);
    u16* o = l == 0 ? x2 : (u16*)d_out;
    k_fin<<<S * D / 256, 256, 0, stream>>>(x1, xn, upd, idx, o);
    cur = o;
  }
}
```

```cpp
#include <hip/hip_runtime.h>
#include <hip/hip_bf16.h>

typedef unsigned short u16;
typedef unsigned u32;
typedef unsigned long long u64;
typedef u32 v4u __attribute__((ext_vector_type(4)));
typedef u32 v2u __attribute__((ext_vector_type(2)));
typedef float v4f __attribute__((ext_vector_type(4)));
typedef short v4s __attribute__((ext_vector_type(4)));
typedef __bf16 v2bf __attribute__((ext_vector_type(2)));
typedef __amdgpu_buffer_rsrc_t rsrc_t;
#define LAS __attribute__((address_space(3)))
#define INL __device__ __forceinline__

struct Params {
  const u16 *x, *Cr, *Ci, *Dr, *Di, *wr, *wg, *wu, *wd;
  u16* out;
  char* ws;
};

constexpr int KB = 1024, MB = 1 << 20;
constexpr int W_EPOCH = 0, W_F1 = 32 * KB, W_F2 = 64 * KB, W_F3 = 96 * KB, W_CAND2 = 128 * KB, W_CAND4 = 160 * KB, W_DAB = 192 * KB,
              W_UPD = 224 * KB, W_DBG = 512 * KB, W_STAT1 = 1 * MB, W_STAT3 = 1 * MB + 512 * KB, W_PY2 = 2 * MB, W_PY4 = 3 * MB,
              W_XF1 = 4 * MB, W_XF3 = 6 * MB, W_ABF1 = 8 * MB, W_ABF2 = 12 * MB, W_EXC1 = 16 * MB, W_EXC2 = 25 * MB, W_END = 34 * MB;
constexpr int L_D = 0, L_RED = 64 * KB, L_STG = 64 * KB, L_WG = 112 * KB, L_WU = L_WG + 8 * KB + 128, L_WD = 129 * KB, L_MISC = 144 * KB;
constexpr int L_RS = L_MISC;
constexpr int L_TR = L_MISC + 256;
constexpr int L_H = L_TR + 4 * KB;
constexpr int L_X = L_H + 256;
constexpr int L_STAMP = L_X + KB;
constexpr int L_BYTES = L_STAMP + 512;
constexpr u32 SPIN_CAP = 1u << 17;
constexpr float EPS = 1e-5f;
constexpr int SC1 = 16;

template <int N> struct IC { static constexpr int v = N; };
template <int A, int B, class F> INL void sfor(F&& f) {
  if constexpr (A < B) { f(IC<A>{}); sfor<A + 1, B>(f); }
}

INL rsrc_t mk(const void* p, int bytes) { return __builtin_amdgcn_make_buffer_rsrc((void*)p, 0, bytes, 0x00020000); }
INL int rfl(int v) { return __builtin_amdgcn_readfirstlane(v); }
INL float rflf(float v) { return __builtin_bit_cast(float, __builtin_amdgcn_readfirstlane(__builtin_bit_cast(int, v))); }
INL v4u ld16c(rsrc_t r, int vo, int so) { return __builtin_amdgcn_raw_buffer_load_b128(r, vo, so, SC1); }
INL v4u ld16(rsrc_t r, int vo, int so) { return __builtin_amdgcn_raw_buffer_load_b128(r, vo, so, 0); }
INL void st16c(v4u v, rsrc_t r, int vo, int so) { __builtin_amdgcn_raw_buffer_store_b128(v, r, vo, so, SC1); asm volatile("s_nop 1" ::"v"(v) : "memory"); }
INL void st16(v4u v, rsrc_t r, int vo, int so) { __builtin_amdgcn_raw_buffer_store_b128(v, r, vo, so, 0); asm volatile("s_nop 1" ::"v"(v) : "memory"); }
INL float asf(u32 u) { return __builtin_bit_cast(float, u); }
INL u32 asu(float f) { return __builtin_bit_cast(u32, f); }
INL float lo16(u32 u) { return asf(u << 16); }
INL float hi16(u32 u) { return asf(u & 0xffff0000u); }
INL u32 pk(float a, float b) { v2bf p = {(__bf16)a, (__bf16)b}; return __builtin_bit_cast(u32, p); }
INL float rbf(float a) { return lo16(pk(a, a)); }
#define CLOB() asm volatile("" ::: "memory")
#define DRAIN() asm volatile("s_waitcnt vmcnt(0)" ::: "memory")
#define VMW(n) asm volatile("s_waitcnt vmcnt(%0)" ::"i"(n) : "memory")
#define LGK0() asm volatile("s_waitcnt lgkmcnt(0)" ::: "memory")
#define BAR() asm volatile("s_waitcnt lgkmcnt(0)\n\ts_barrier" ::: "memory")
INL void MFMA0(v4f& acc, const v4u& a, const v4u& b) { asm volatile("v_mfma_f32_16x16x32_bf16 %0, %1, %2, 0" : "=&v"(acc) : "v"(a), "v"(b)); }
INL void MFMA(v4f& acc, const v4u& a, const v4u& b) { asm volatile("v_mfma_f32_16x16x32_bf16 %0, %1, %2, %0" : "+v"(acc) : "v"(a), "v"(b)); }
INL void MFMA0A(v4f& acc, const v4u& a, const v4u& b) { asm volatile("v_mfma_f32_16x16x32_bf16 %0, %1, %2, 0" : "=&v"(acc) : "v"(a), "a"(b)); }
INL void MFMAA(v4f& acc, const v4u& a, const v4u& b) { asm volatile("v_mfma_f32_16x16x32_bf16 %0, %1, %2, %0" : "+v"(acc) : "v"(a), "a"(b)); }
template <class T> INL void PIN(T& x) { asm volatile("" : "+v"(x)); }
INL void PINA(v4u& x) { asm volatile("" : "+a"(x)); }
INL void VADD(float& d, float s) { asm volatile("v_add_f32 %0, %0, %1" : "+v"(d) : "v"(s)); }
INL void VADD3(float& d, float a, float b) { asm volatile("v_add_f32 %0, %1, %2" : "=v"(d) : "v"(a), "v"(b)); }

INL void dma_unit(int lds, const int (&vo)[4], rsrc_t r, int so) {
  asm volatile(
      "s_mov_b32 m0, %0\n\ts_nop 0\n\t"
      "buffer_load_dwordx4 %1, %5, %6 offen lds\n\t"
      "buffer_load_dwordx4 %2, %5, %6 offen offset:1024 lds\n\t"
      "buffer_load_dwordx4 %3, %5, %6 offen offset:2048 lds\n\t"
      "buffer_load_dwordx4 %4, %5, %6 offen offset:3072 lds"
      ::"s"(lds), "v"(vo[0]), "v"(vo[1]), "v"(vo[2]), "v"(vo[3]), "s"(r), "s"(so) : "memory");
}
INL void dma_one(int lds, int vo, rsrc_t r, int so) {
  asm volatile("s_mov_b32 m0, %0\n\ts_nop 0\n\tbuffer_load_dwordx4 %1, %2, %3 offen lds" ::"s"(lds), "v"(vo), "s"(r), "s"(so) : "memory");
}

template <int CTRL> INL u32 dppu(u32 v) { return (u32)__builtin_amdgcn_update_dpp(0, (int)v, CTRL, 0xf, 0xf, true); }
template <int CTRL> INL float dppf(float v) { return asf(dppu<CTRL>(asu(v))); }
INL u32 umax(u32 a, u32 b) { return a > b ? a : b; }
INL u32 umin(u32 a, u32 b) { return a < b ? a : b; }
INL u32 row_max(u32 v) { v = umax(v, dppu<0x128>(v)); v = umax(v, dppu<0x124>(v)); v = umax(v, dppu<0x122>(v)); return umax(v, dppu<0x121>(v)); }
INL float row_sum(float v) { v += dppf<0x128>(v); v += dppf<0x124>(v); v += dppf<0x122>(v); return v + dppf<0x121>(v); }
INL u32 max16(u32 v) { auto s = __builtin_amdgcn_permlane16_swap(v, v, false, false); return umax(s[0], s[1]); }
INL u32 max32(u32 v) { auto s = __builtin_amdgcn_permlane32_swap(v, v, false, false); return umax(s[0], s[1]); }
INL float sum16(float v) { auto s = __builtin_amdgcn_permlane16_swap(asu(v), asu(v), false, false); return asf(s[0]) + asf(s[1]); }
INL float sum32(float v) { auto s = __builtin_amdgcn_permlane32_swap(asu(v), asu(v), false, false); return asf(s[0]) + asf(s[1]); }
INL float wave_sum(float v) { return sum32(sum16(row_sum(v))); }
INL float red_g(float a, float b) { auto s = __builtin_amdgcn_permlane16_swap(asu(a), asu(b), false, false); return sum32(asf(s[0]) + asf(s[1])); }

#define STAMP(i)

struct Sel { int t0, t1; float m0, m1, s0, s1, r0, r1; };

INL void wait_flags(rsrc_t ws, int vo, int n, int lane, u32 tlo, u32 thi) {
  for (u32 sp = 0; sp < SPIN_CAP; ++sp) {
    CLOB();
    v2u f = {tlo, thi};
    if (lane < n) f = __builtin_amdgcn_raw_buffer_load_b64(ws, vo, 0, SC1);
    if (__all(f.x == tlo && f.y == thi)) break;
  }
  asm volatile("" ::: "memory");
}
INL void gather_stats(rsrc_t ws, int so, int lane, u32 tag, float& S, float& Q, float& Dt) {
  for (u32 sp = 0; sp < SPIN_CAP; ++sp) {
    CLOB();
    v4u v[8];
#pragma unroll
    for (int i = 0; i < 8; ++i) v[i] = ld16c(ws, lane * 16, so + i * 1024);
    bool ok = true;
    S = 0.f; Q = 0.f; Dt = 0.f;
#pragma unroll
    for (int i = 0; i < 8; ++i) { ok &= v[i].w == tag; S += asf(v[i].x); Q += asf(v[i].y); Dt += asf(v[i].z); }
    if (__all(ok)) break;
  }
  S = sum32(S); Q = sum32(Q); Dt = sum32(Dt);
}
INL u32 mkkey(float r, int t) {
  u32 b = pk(r, r) & 0xffffu;
  b = (b & 0x8000u) ? (b ^ 0xffffu) : (b | 0x8000u);
  return (b << 16) | (u32)(2047 - t);
}
INL float key_r(u32 k) { u32 b = k >> 16; b = (b & 0x8000u) ? (b & 0x7fffu) : (b ^ 0xffffu); return asf(b << 16); }
INL void publish_cand(rsrc_t ws, int so, int lane, u32 tag, int tb, float r, float mean, float rs) {
  const int c = lane & 15, mt = (lane >> 4) & 1;
  const u32 key = mkkey(r, 32 * tb + 8 * (c >> 2) + 4 * mt + (c & 3));
  const u32 m1 = max16(row_max(key));
  const u32 m2 = max16(row_max(key == m1 ? 0u : key));
  if (lane < 32 && (key == m1 || key == m2)) st16c(v4u{asu(mean), asu(rs), key, tag}, ws, key == m1 ? 0 : 16, so);
}
INL Sel select_top2(rsrc_t ws, int so, int lane, u32 tag) {
  v4u a, b;
  for (u32 sp = 0; sp < SPIN_CAP; ++sp) {
    CLOB();
    a = ld16c(ws, lane * 16, so);
    b = ld16c(ws, lane * 16, so + 1024);
    if (__all(a.w == tag && b.w == tag)) break;
  }
  const u32 hi = umax(a.z, b.z), lo = umin(a.z, b.z);
  const u32 m1 = max32(max16(row_max(hi)));
  const u32 m2 = max32(max16(row_max(hi == m1 ? lo : hi)));
  const int l1 = __builtin_ctzll(__ballot(hi == m1) | (1ull << 63)), l2 = __builtin_ctzll(__ballot(hi == m2 || lo == m2) | (1ull << 63));
  const u32 mA = a.z == m1 ? a.x : b.x, sA = a.z == m1 ? a.y : b.y, mB = a.z == m2 ? a.x : b.x, sB = a.z == m2 ? a.y : b.y;
  Sel s;
  const u32 k1 = (u32)rfl((int)m1), k2 = (u32)rfl((int)m2);
  s.t0 = 2047 - (int)(k1 & 2047u); s.t1 = 2047 - (int)(k2 & 2047u);
  s.r0 = key_r(k1); s.r1 = key_r(k2);
  s.m0 = asf(__builtin_amdgcn_readlane(mA, l1)); s.s0 = asf(__builtin_amdgcn_readlane(sA, l1));
  s.m1 = asf(__builtin_amdgcn_readlane(mB, l2)); s.s1 = asf(__builtin_amdgcn_readlane(sB, l2));
  return s;
}
INL int xf_tok(int t) { const int r = t & 31; return ((t >> 5) * 2 + ((r >> 2) & 1)) * 16384 + (4 * (r >> 3) + (r & 3)) * 16; }
INL u32 tagb(float v, u32 tag, int i) { return (asu(v) & 0xffffff00u) | ((tag >> (8 * i)) & 0xffu); }
INL u32 tagdiff(v4u v, u32 tag) { return ((v.x ^ tag) | (v.y ^ (tag >> 8)) | (v.z ^ (tag >> 16)) | (v.w ^ (tag >> 24))) & 0xffu; }
INL float sigm(float r) { return rbf(1.f / (1.f + __expf(-r))); }

__global__ void __launch_bounds__(256, 1) fnet_mod_kernel(Params P) {
  extern __shared__ __attribute__((aligned(16))) char smem[];
  LAS char* L = (LAS char*)smem;
  int tid = threadIdx.x, lane = tid & 63;
  const int w = rfl(tid >> 6);
  const int b = blockIdx.x, xcd = b & 7, loc = b >> 3;
  const int k2 = xcd & 3, p2 = (xcd >> 2) * 8 + (loc >> 2), j2 = loc & 3, ec = 4 * j2 + k2;
  const int p1 = 4 * (xcd >> 1) + (loc >> 3), i1 = 8 * (xcd & 1) + (loc & 7);
  const int fline = xcd + 8 * (loc >> 3), cg = fline * 8 + (loc & 7), rho = (xcd - 4) * 8 + (loc & 7);
  const bool isGU = fline < 28;
  int c = lane & 15, g = lane >> 4, l16 = lane * 16, tok0 = 0;
  const int tb2 = 4 * p2 + ((w + j2) & 3);
  auto fresh = [&]() { PIN(l16); lane = l16 >> 4; tid = w * 64 + lane; c = lane & 15; g = lane >> 4; tok0 = 32 * tb2 + 8 * (c >> 2) + (c & 3); };
  const rsrc_t ws = mk(P.ws, W_END);
  STAMP(0);

  int vo1[4], vo4[4];
  {
    const int rowl = 8 * (lane >> 5) + ((lane >> 3) & 3), ch0 = ((lane & 7) ^ (lane >> 4)) * 16;
#pragma unroll
    for (int i = 0; i < 4; ++i) {
      const int row = rowl + 16 * (i & 1) + 4 * (i >> 1), ch = (ch0 ^ ((i & 1) * 64)) + 3072 - 1024 * i;
      vo1[i] = row * 1024 + ch; vo4[i] = row * 4096 + ch;
    }
  }
  const rsrc_t rD = mk((const char*)(i1 < 8 ? P.Dr : P.Di) + (i1 & 7) * 65536 - 3072, 65536 + 3072);
  const rsrc_t rX = mk((const char*)P.x + (128 * p1 + 32 * w) * 1024 - 3072, 32768 + 3072);
  const rsrc_t rC = mk((const char*)((k2 >> 1) ? P.Ci : P.Cr) + (size_t)p2 * 128 * 4096 + (k2 & 1) * 2048 + w * 512 - 3072, 127 * 4096 + 512 + 3072);
  const int stg = L_STG + w * 20480;
  auto dmaD = [&](int r) { const int idx = w + 4 * ((r + p1) & 3); dma_unit(L_D + idx * 4096, vo1, rD, (idx >> 3) * 32768 + (idx & 7) * 128); };
  auto dmaX = [&](int u) { dma_unit(stg + (u % 5) * 4096, vo1, rX, ((u + i1) & 7) * 128); };
  auto dmaC = [&](int n) { dma_unit(stg + ((8 + n) % 5) * 4096, vo4, rC, ((w + j2 + 1 + (n >> 2)) & 3) * 131072 + (n & 3) * 128); };
  asm volatile("s_nop 4" ::: "memory");
  dmaD(0); dmaX(0);
  const v2u ep = __builtin_amdgcn_raw_buffer_load_b64(ws, 0, W_EPOCH, SC1);
  dmaD(1); dmaD(2); dmaD(3);
  dmaX(1); dmaX(2); dmaX(3); dmaX(4);
  int A0 = c * 128 + ((g ^ (c >> 1)) * 16), A1 = A0 ^ 64;
  v4u ones = {0x3f803f80u, 0x3f803f80u, 0x3f803f80u, 0x3f803f80u};
  PIN(ones);
  VMW(16);
  asm volatile("s_barrier" ::: "memory");
  STAMP(1);
  {
    v4f r;
    const int base = L_D + (w >> 1) * 32768 + (w & 1) * 2048;
    sfor<0, 16>([&](auto I) {
      constexpr int i = I.v;
      v4u d = *(const LAS v4u*)(L + base + ((i & 1) ? A1 : A0) + (i >> 1) * 4096);
      if constexpr (i == 0) MFMA0(r, ones, d); else MFMA(r, ones, d);
    });
    asm volatile("s_nop 7" : "+v"(r));
    if (g == 0) *(LAS float*)(L + L_RS + (w * 16 + c) * 4) = r[0];
  }
  v4f acc[8], accS[2], accQ[2];
  sfor<0, 8>([&](auto U) {
    constexpr int u = U.v;
    VMW(16);
    v4u xf[4], df[8];
    const int sl = stg + (u % 5) * 4096, dk = L_D + ((u + i1) & 7) * 4096;
#pragma unroll
    for (int i = 0; i < 4; ++i) xf[i] = *(const LAS v4u*)(L + sl + ((i & 1) ? A1 : A0) + (i >> 1) * 2048);
#pragma unroll
    for (int i = 0; i < 4; ++i) PIN(xf[i]);
    LGK0();
    if constexpr (u + 5 < 8) dmaX(u + 5); else dmaC(u + 5 - 8);
#pragma unroll
    for (int i = 0; i < 8; ++i) df[i] = *(const LAS v4u*)(L + dk + ((i >> 2) ? A1 : A0) + ((i >> 1) & 1) * 32768 + (i & 1) * 2048);
    sfor<0, 2>([&](auto KB_) {
      constexpr int kb = KB_.v;
      sfor<0, 2>([&](auto MT) {
        constexpr int mt = MT.v;
        if constexpr (u == 0 && kb == 0) { MFMA0(accS[mt], xf[mt * 2 + kb], ones); MFMA0(accQ[mt], xf[mt * 2 + kb], xf[mt * 2 + kb]); }
        else { MFMA(accS[mt], xf[mt * 2 + kb], ones); MFMA(accQ[mt], xf[mt * 2 + kb], xf[mt * 2 + kb]); }
      });
      sfor<0, 8>([&](auto I) {
        constexpr int i = I.v, mt = i & 1, nn = i >> 1;
        if constexpr (u == 0 && kb == 0) MFMA0(acc[mt * 4 + nn], xf[mt * 2 + kb], df[kb * 4 + nn]);
        else MFMA(acc[mt * 4 + nn], xf[mt * 2 + kb], df[kb * 4 + nn]);
      });
    });
  });
  asm volatile("s_nop 7" : "+v"(acc[0]), "+v"(acc[1]), "+v"(acc[2]), "+v"(acc[3]), "+v"(acc[4]), "+v"(acc[5]), "+v"(acc[6]), "+v"(acc[7]),
               "+v"(accS[0]), "+v"(accS[1]), "+v"(accQ[0]), "+v"(accQ[1]));
  BAR();
  STAMP(2);
  float RS[4];
#pragma unroll
  for (int i = 0; i < 4; ++i) RS[i] = *(const LAS float*)(L + L_RS + (i * 16 + c) * 4);
  const float sgn = i1 < 8 ? 1.f : -1.f;
  auto gemm1_out = [&](const float (&mean)[8], const float (&sc)[8], int base) {
#pragma unroll
    for (int nn = 0; nn < 4; ++nn) {
      float v[8];
#pragma unroll
      for (int q = 0; q < 8; ++q) v[q] = (acc[(q >> 2) * 4 + nn][q & 3] - mean[q] * RS[nn]) * sc[q];
      st16c(v4u{pk(v[0], v[1]), pk(v[2], v[3]), pk(v[4], v[5]), pk(v[6], v[7])}, ws, l16, base + ((4 * i1 + nn) * 64 + 4 * p1 + w) * 1024);
    }
  };
  {
    float mean[8], sc[8];
#pragma unroll
    for (int q = 0; q < 8; ++q) {
      const float Q = asf(__builtin_amdgcn_ds_bpermute((20 * g + (q & 3)) * 4, asu(accQ[q >> 2][q & 3])));
      mean[q] = accS[q >> 2][q & 3] * (1.f / 512);
      sc[q] = sgn * __builtin_amdgcn_rsqf(fmaxf(Q * (1.f / 512) - mean[q] * mean[q], 0.f) + EPS);
    }
    gemm1_out(mean, sc, W_ABF1);
  }
  const u64 epoch = (((u64)(u32)rfl((int)ep.y) << 32) | (u32)rfl((int)ep.x)) + 1;
  const u32 tag = (u32)epoch, tagh = (u32)(epoch >> 32);
  {
    const v4u z = {tag - 1, tag - 1, tag - 1, tag - 1};
    if (w == 0 && lane < 40) {
      const int i = lane >> 3;
      const int off = i == 0 ? W_F2 + (p2 * 16 + ec) * 128 : i == 1 ? W_F3 + (p1 * 16 + i1) * 128 : i == 2 ? W_CAND2 + (i1 * 16 + p1) * 128
                    : i == 3 ? W_CAND4 + (ec * 16 + p2) * 128 : W_DAB + b * 128;
      st16c(z, ws, off + (lane & 7) * 16, 0);
    }
  }
  STAMP(3);
  v4u cf[64];
  DRAIN();
  asm volatile("s_barrier" ::: "memory");
  if (tid < 8) st16c(v4u{tag, tagh, tag, tagh}, ws, l16, W_F1 + (p1 * 16 + i1) * 128);
  STAMP(4);
  sfor<0, 16>([&](auto N) {
    constexpr int n = N.v;
    if constexpr (n >= 5) VMW(n + 4 < 16 ? 16 : (15 - n) * 4);
    const int sl = stg + ((8 + n) % 5) * 4096;
    sfor<0, 4>([&](auto I) {
      constexpr int i = I.v;
      cf[(n >> 2) * 16 + (i >> 1) * 8 + (n & 3) * 2 + (i & 1)] = *(const LAS v4u*)(L + sl + ((i & 1) ? A1 : A0) + (i >> 1) * 2048);
    });
    sfor<0, 4>([&](auto I) { constexpr int i = I.v; PINA(cf[(n >> 2) * 16 + (i >> 1) * 8 + (n & 3) * 2 + (i & 1)]); });
    LGK0();
    if constexpr (n + 5 < 16) dmaC(n + 5);
  });
  STAMP(5);

  auto fvo = [&]() { return ((8 * (k2 & 1) + 2 * w + ((lane >> 1) & 1)) * 16 + 8 * (k2 >> 1) + 2 * j2 + (lane & 1)) * 128; };

  auto gemm2 = [&](int abf, int exc, float (&y)[16], auto&& hook) {
    int redW[3];
#pragma unroll
    for (int u = 0; u < 3; ++u) redW[u] = L_RED + (((w + 1 + u) & 3) * 3 + u) * 4096 + l16;
    const int redR = L_RED + w * 12288 + l16;
    v4u ab[2][16];
    v4f a[2][4];
    float base[16], bx[16];
    v4u xe[9];
    auto abso = [&](int i, int f) { return abf + (((16 * (k2 >> 1) + 4 * j2 + ((k2 + 1 + i) & 3)) * 2 + (f >> 3)) * 64 + 32 * (k2 & 1) + 8 * w + (f & 7)) * 1024; };
    const int excin = exc + ((p2 * 16 + ec) * 12 + w) * 3072;
#pragma unroll
    for (int f = 0; f < 16; ++f) ab[0][f] = ld16c(ws, l16, abso(0, f));
    sfor<0, 4>([&](auto I) {
      constexpr int i = I.v;
      v4f rd[12];
      u32 pkd[8];
      sfor<0, 128>([&](auto N) {
        constexpr int n = N.v, u = n >> 5, ch = (n >> 3) & 3, ks = n & 7;
        if constexpr (ks == 0) MFMA0A(a[u & 1][ch], ab[i & 1][(ch & 1) * 8 + ks], cf[u * 16 + (ch >> 1) * 8 + ks]);
        else MFMAA(a[u & 1][ch], ab[i & 1][(ch & 1) * 8 + ks], cf[u * 16 + (ch >> 1) * 8 + ks]);
        if constexpr (i > 0 && n >= 8 && n < 24) VADD(base[n - 8], a[1][(n - 8) >> 2][(n - 8) & 3]);
        if constexpr (i > 0 && n >= 18 && n < 26) { pkd[n - 18] = pk(base[2 * (n - 18)], base[2 * (n - 18) + 1]); PIN(pkd[n - 18]); }
        if constexpr (i > 0 && n >= 26 && n < 29) {
          constexpr int q = n - 26;
          st16c(v4u{pkd[3 * q], pkd[3 * q + 1], q < 2 ? pkd[(3 * q + 2) & 7] : tag, tag}, ws, l16,
                exc + (((p2 * 16 + 4 * j2 + ((k2 + i) & 3)) * 3 + (i - 1)) * 4 + w) * 3072 + q * 1024);
        }
        if constexpr (n == 14) BAR();
        if constexpr (n >= 15 && n <= 103 && (n & 7) == 7) {
          constexpr int m = (n - 15) >> 3;
          PIN(a[(m >> 2) & 1][m & 3]);
          *(LAS v4f*)(L + redW[m >> 2] + (m & 3) * 1024) = a[(m >> 2) & 1][m & 3];
        }
        if constexpr (n == 105) BAR();
        if constexpr (n >= 105 && n < 117) rd[n - 105] = *(const LAS v4f*)(L + redR + (((n - 105) % 3) * 4 + (n - 105) / 3) * 1024);
        if constexpr (n >= 112) {
          constexpr int q = n - 112;
          VADD3(base[q], rd[(q >> 2) * 3][q & 3], rd[(q >> 2) * 3 + 1][q & 3]);
          VADD(base[q], rd[(q >> 2) * 3 + 2][q & 3]);
        }
        if constexpr (i < 3 && n >= 30 && n < 94 && (n & 3) == 2) ab[(i + 1) & 1][(n - 30) >> 2] = ld16c(ws, l16, abso(i + 1, (n - 30) >> 2));
        if constexpr (i == 3 && n >= 30 && n < 36) xe[n - 30] = ld16c(ws, l16, excin + ((n - 30) / 3) * 12288 + ((n - 30) % 3) * 1024);
        if constexpr (i == 3 && n >= 72 && n < 75) xe[n - 66] = ld16c(ws, l16, excin + 2 * 12288 + (n - 72) * 1024);
        hook(I, N);
      });
    });
    bool ok = true;
#pragma unroll
    for (int q = 0; q < 9; ++q) ok &= xe[q].w == tag;
    for (u32 sp = 0; !__all(ok) && sp < SPIN_CAP; ++sp) {
      CLOB();
      ok = true;
#pragma unroll
      for (int q = 0; q < 9; ++q) xe[q] = ld16c(ws, l16, excin + (q / 3) * 12288 + (q % 3) * 1024);
#pragma unroll
      for (int q = 0; q < 9; ++q) ok &= xe[q].w == tag;
    }
#pragma unroll
    for (int q = 0; q < 8; ++q) {
      const u32 d0 = xe[q / 3][q % 3], d1 = xe[3 + q / 3][q % 3], d2 = xe[6 + q / 3][q % 3];
      bx[2 * q] = lo16(d0) + lo16(d1) + lo16(d2);
      bx[2 * q + 1] = hi16(d0) + hi16(d1) + hi16(d2);
    }
    asm volatile("s_nop 7" : "+v"(a[1][0]), "+v"(a[1][1]), "+v"(a[1][2]), "+v"(a[1][3]));
#pragma unroll
    for (int q = 0; q < 16; ++q) y[q] = (a[1][q >> 2][q & 3] + base[q]) + bx[q];
  };
  const rsrc_t rW = mk(P.wr, 1024);
  auto tile_out = [&](const float (&v)[16], u32 (&xp)[8], int xf, int stat, bool drain_first) {
    const v4u wq = ld16(rW, g * 16, ec * 64);
#pragma unroll
    for (int q = 0; q < 8; ++q) xp[q] = pk(v[2 * q], v[2 * q + 1]);
    st16c(v4u{xp[0], xp[1], xp[2], xp[3]}, ws, l16, xf + ((tb2 * 2 + 0) * 16 + ec) * 1024);
    st16c(v4u{xp[4], xp[5], xp[6], xp[7]}, ws, l16, xf + ((tb2 * 2 + 1) * 16 + ec) * 1024);
    float s[2], q2[2], d[2];
#pragma unroll
    for (int mt = 0; mt < 2; ++mt) {
      s[mt] = 0.f; q2[mt] = 0.f; d[mt] = 0.f;
#pragma unroll
      for (int q = 0; q < 4; ++q) {
        const float a0 = lo16(xp[mt * 4 + q]), a1 = hi16(xp[mt * 4 + q]);
        s[mt] += a0 + a1; q2[mt] += a0 * a0 + a1 * a1; d[mt] += a0 * lo16(wq[q]) + a1 * hi16(wq[q]);
      }
    }
    const float S = red_g(s[0], s[1]), Q = red_g(q2[0], q2[1]), Dt = red_g(d[0], d[1]);
    if (drain_first) DRAIN();
    if (lane < 32) st16c(v4u{asu(S), asu(Q), asu(Dt), tag}, ws, l16, stat + (tb2 * 16 + ec) * 512);
  };

  fresh();
  wait_flags(ws, W_F1 + fvo(), 4, lane, tag, tagh);
  STAMP(6);
  u32 x1p[8];
  {
    float y[16];
    v4u xr[2];
    const rsrc_t rXa = mk(P.x, 2 * MB);
    const rsrc_t rG = mk((const char*)((w >> 1) ? P.wu : P.wg) + cg * 16, isGU ? 512 * 3584 - cg * 16 : 0);
    const rsrc_t rWd = mk((const char*)P.wd + cg * 8192, isGU ? 8192 : 0);
    const int pos = lane & 31;
    const int gvo = (32 * (lane >> 5) + 8 * ((pos >> 2) & 3) + 4 * (pos >> 4) + (pos & 3)) * 3584;
    gemm2(W_ABF1, W_EXC1, y, [&](auto I, auto N) {
      constexpr int i = I.v, n = N.v;
      if constexpr (i == 3 && (n == 40 || n == 41)) xr[n - 40] = ld16(rXa, (tok0 + 4 * (n - 40)) * 1024 + g * 16, ec * 64);
      if constexpr ((i == 1 || i == 2) && (n == 96 || n == 100)) {
        constexpr int q = (i - 1) * 2 + (n - 96) / 4;
        dma_one(((w >> 1) ? L_WU : L_WG) + ((w & 1) * 4 + q) * 1024, gvo, rG, ((w & 1) * 4 + q) * 64 * 3584);
      }
      if constexpr ((i == 1 || i == 2) && n == 98) dma_one(L_WD + (w * 2 + i - 1) * 1024, l16, rWd, (w * 2 + i - 1) * 1024);
    });
    STAMP(7);
    fresh();
#pragma unroll
    for (int q = 0; q < 8; ++q) { y[2 * q] += lo16(xr[q >> 2][q & 3]); y[2 * q + 1] += hi16(xr[q >> 2][q & 3]); }
    tile_out(y, x1p, W_XF1, W_STAT1, false);
  }
  DRAIN();
  asm volatile("s_barrier" ::: "memory");
  if (tid < 8) st16c(v4u{tag, tagh, tag, tagh}, ws, l16, W_F2 + (p2 * 16 + ec) * 128);
  STAMP(8);
  float Wsum;
  {
    const v4u wq = ld16(rW, l16, 0);
    float s = 0.f;
#pragma unroll
    for (int q = 0; q < 4; ++q) s += lo16(wq[q]) + hi16(wq[q]);
    Wsum = rflf(wave_sum(s));
  }
  auto wfrag = [&](int kb) {
    const int trA = ((c >> 1) & 1 ? L_WU : L_WG) + (4 * g + (c >> 2)) * 16 + (c & 1) * 8;
    const v4s lo = __builtin_amdgcn_ds_read_tr16_b64_v4i16((LAS v4s*)(L + trA + kb * 512));
    const v4s hi = __builtin_amdgcn_ds_read_tr16_b64_v4i16((LAS v4s*)(L + trA + kb * 512 + 256));
    const v2u a = __builtin_bit_cast(v2u, lo), bb = __builtin_bit_cast(v2u, hi);
    return v4u{a.x, a.y, bb.x, bb.y};
  };
  auto expert_front = [&](int cand, int xf, int py) {
    if (w == 0) {
      const Sel s = select_top2(ws, cand + (b & 15) * 2048, lane, tag);
      if (isGU) {
        v4u xr[16];
        const int vo = ((c & 1) ? xf_tok(s.t1) : xf_tok(s.t0)) + g * 256;
#pragma unroll
        for (int kb = 0; kb < 16; ++kb) xr[kb] = ld16c(ws, vo, xf + kb * 1024);
        v4f h, cs;
        ones = v4u{0x3f803f80u, 0x3f803f80u, 0x3f803f80u, 0x3f803f80u};
        PIN(ones);
        sfor<0, 16>([&](auto K) {
          constexpr int kb = K.v;
          v4u wf = wfrag(kb);
          asm volatile("s_nop 1" : "+v"(wf));
          if constexpr (kb == 0) { MFMA0(cs, wf, ones); MFMA0(h, wf, xr[kb]); } else { MFMA(cs, wf, ones); MFMA(h, wf, xr[kb]); }
        });
        asm volatile("s_nop 7" : "+v"(h), "+v"(cs));
        const float mean = (c & 1) ? s.m1 : s.m0, rs = (c & 1) ? s.s1 : s.s0;
        v4f o;
#pragma unroll
        for (int i = 0; i < 4; ++i) {
          const float v = rbf((h[i] - mean * cs[i]) * rs);
          const auto sw = __builtin_amdgcn_permlane32_swap(asu(v), asu(v), false, false);
          const float gt = asf(sw[0]), up = asf(sw[1]);
          o[i] = rbf(rbf(gt / (1.f + __expf(-gt))) * up);
        }
        if (c < 2 && g < 2) *(LAS v4f*)(L + L_H + c * 32 + g * 16) = o;
      }
      if (lane == 0) {
        *(LAS v4u*)(L + L_H + 64) = v4u{(u32)s.t0, (u32)s.t1, asu(s.m0), asu(s.m1)};
        *(LAS v4u*)(L + L_H + 80) = v4u{asu(s.s0), asu(s.s1), asu(s.r0), asu(s.r1)};
      }
    }
    u32 wd[8];
#pragma unroll
    for (int i = 0; i < 8; ++i) wd[i] = *(const LAS u32*)(L + L_WD + i * 1024 + tid * 4);
    BAR();
    if (isGU) {
      float y[4] = {0.f, 0.f, 0.f, 0.f};
#pragma unroll
      for (int q = 0; q < 4; ++q) {
        const v4f h = *(const LAS v4f*)(L + L_H + q * 16);
#pragma unroll
        for (int i = 0; i < 4; ++i) { y[(q >> 1) * 2] += h[i] * lo16(wd[(q & 1) * 4 + i]); y[(q >> 1) * 2 + 1] += h[i] * hi16(wd[(q & 1) * 4 + i]); }
      }
      st16c(v4u{tagb(y[0], tag, 0), tagb(y[1], tag, 1), tagb(y[2], tag, 2), tagb(y[3], tag, 3)}, ws, tid * 16, py + cg * 4096);
    }
    const v4u s0 = *(const LAS v4u*)(L + L_H + 64), s1 = *(const LAS v4u*)(L + L_H + 80);
    Sel s;
    s.t0 = rfl((int)s0.x); s.t1 = rfl((int)s0.y); s.m0 = rflf(asf(s0.z)); s.m1 = rflf(asf(s0.w));
    s.s0 = rflf(asf(s1.x)); s.s1 = rflf(asf(s1.y)); s.r0 = rflf(asf(s1.z)); s.r1 = rflf(asf(s1.w));
    return s;
  };
  auto expert_back = [&](int py, const Sel& s, float (&u)[4]) {
    v4f t;
    for (u32 sp = 0; sp < SPIN_CAP; ++sp) {
    CLOB();
      v4u v[7];
#pragma unroll
      for (int i = 0; i < 7; ++i) v[i] = ld16c(ws, (tid >> 3) * 4096 + (tid & 7) * 16, py + rho * 128 + i * 131072);
      u32 bad = 0;
      t = v4f{0.f, 0.f, 0.f, 0.f};
#pragma unroll
      for (int i = 0; i < 7; ++i) { bad |= tagdiff(v[i], tag); t += __builtin_bit_cast(v4f, v[i]); }
      if (__all(bad == 0)) break;
    }
#pragma unroll
    for (int i = 0; i < 4; ++i) t[i] = sum32(sum16(t[i] + dppf<0x128>(t[i])));
    if (lane < 8) *(LAS v4f*)(L + L_X + w * 128 + l16) = t;
    BAR();
    t = v4f{0.f, 0.f, 0.f, 0.f};
#pragma unroll
    for (int i = 0; i < 4; ++i) t += *(const LAS v4f*)(L + L_X + i * 128 + (lane & 7) * 16);
    const float g0 = sigm(s.r0), g1 = sigm(s.r1);
    u[0] = rbf(rbf(t[0]) * g0); u[1] = rbf(rbf(t[1]) * g0); u[2] = rbf(rbf(t[2]) * g1); u[3] = rbf(rbf(t[3]) * g1);
  };

  DRAIN();
  if (w == 0) wait_flags(ws, W_F2 + (p1 * 16 + c) * 128, 16, lane, tag, tagh);
  asm volatile("s_barrier" ::: "memory");
  STAMP(9);
  {
    const int tb1 = 4 * p1 + w;
    A0 = c * 128 + ((g ^ (c >> 1)) * 16); A1 = A0 ^ 64;
#pragma unroll
    for (int i = 0; i < 4; ++i) RS[i] = *(const LAS float*)(L + L_RS + (i * 16 + c) * 4);
    v4u sv[8], xf[32];
#pragma unroll
    for (int i = 0; i < 8; ++i) sv[i] = ld16c(ws, l16, W_STAT1 + tb1 * 8192 + i * 1024);
#pragma unroll
    for (int i = 0; i < 32; ++i) xf[i] = ld16c(ws, l16, W_XF1 + ((tb1 * 2 + ((i >> 1) & 1)) * 16 + 2 * (((i >> 2) + i1) & 7) + (i & 1)) * 1024);
    __builtin_amdgcn_sched_barrier(0);
    float S = 0.f, Q = 0.f, Dt = 0.f;
#pragma unroll
    for (int i = 0; i < 8; ++i) { S += asf(sv[i].x); Q += asf(sv[i].y); Dt += asf(sv[i].z); }
    S = sum32(S); Q = sum32(Q); Dt = sum32(Dt);
    const float mean = S * (1.f / 512), var = fmaxf(Q * (1.f / 512) - mean * mean, 0.f), rs = __builtin_amdgcn_rsqf(var + EPS);
    publish_cand(ws, W_CAND2 + (i1 * 16 + p1) * 128 + w * 32, lane, tag, tb1, (Dt - mean * Wsum) * rs, mean, rs);
    const float c3 = (1.f + rs) * __builtin_amdgcn_rsqf(var * (1.f + rs) * (1.f + rs) + EPS);
    if (lane < 32) { *(LAS float*)(L + L_TR + w * 1024 + lane * 4) = mean; *(LAS float*)(L + L_TR + w * 1024 + 128 + lane * 4) = c3 * sgn; }
    sfor<0, 8>([&](auto U) {
      constexpr int u = U.v;
      v4u df[8];
      const int dk = L_D + ((u + i1) & 7) * 4096;
#pragma unroll
      for (int i = 0; i < 8; ++i) df[i] = *(const LAS v4u*)(L + dk + ((i >> 2) ? A1 : A0) + ((i >> 1) & 1) * 32768 + (i & 1) * 2048);
      sfor<0, 16>([&](auto I) {
        constexpr int i = I.v, kb = i >> 3, mt = i & 1, nn = (i >> 1) & 3;
        if constexpr (u == 0 && kb == 0) MFMA0(acc[mt * 4 + nn], xf[u * 4 + mt * 2 + kb], df[kb * 4 + nn]);
        else MFMA(acc[mt * 4 + nn], xf[u * 4 + mt * 2 + kb], df[kb * 4 + nn]);
      });
    });
    float mn[8], sc[8];
#pragma unroll
    for (int mt = 0; mt < 2; ++mt) {
      const v4f a = *(const LAS v4f*)(L + L_TR + w * 1024 + mt * 64 + g * 16), bb = *(const LAS v4f*)(L + L_TR + w * 1024 + 128 + mt * 64 + g * 16);
#pragma unroll
      for (int i = 0; i < 4; ++i) { mn[mt * 4 + i] = a[i]; sc[mt * 4 + i] = bb[i]; }
    }
    asm volatile("s_nop 7" : "+v"(acc[0]), "+v"(acc[1]), "+v"(acc[2]), "+v"(acc[3]), "+v"(acc[4]), "+v"(acc[5]), "+v"(acc[6]), "+v"(acc[7]));
    STAMP(10);
    gemm1_out(mn, sc, W_ABF2);
  }
  DRAIN();
  asm volatile("s_barrier" ::: "memory");
  if (tid < 8) st16c(v4u{tag, tagh, tag, tagh}, ws, l16, W_F3 + (p1 * 16 + i1) * 128);
  STAMP(11);

  const Sel s2 = expert_front(W_CAND2, W_XF1, W_PY2);
  STAMP(12);
  if (!isGU) {
    float u[4];
    expert_back(W_PY2, s2, u);
    if (tid < 8) st16c(v4u{tagb(u[0], tag, 0), tagb(u[1], tag, 1), tagb(u[2], tag, 2), tagb(u[3], tag, 3)}, ws, l16, W_UPD + rho * 128);
  }
  {
    const int n = 4 * b + w;
    const rsrc_t rDn = mk((const char*)(n < 512 ? P.Dr : P.Di) + (n & 511) * 1024, 1024);
    u32 dn[4], xa[4], xb[4];
    const int xo = (lane >> 4) * 1024 + ((lane >> 2) & 3) * 256 + (lane & 3) * 4;
#pragma unroll
    for (int i = 0; i < 4; ++i) {
      dn[i] = __builtin_amdgcn_raw_buffer_load_b32(rDn, lane * 4, i * 256, 0);
      xa[i] = __builtin_amdgcn_raw_buffer_load_b32(ws, xo, W_XF1 + xf_tok(s2.t0) + i * 4096, SC1);
      xb[i] = __builtin_amdgcn_raw_buffer_load_b32(ws, xo, W_XF1 + xf_tok(s2.t1) + i * 4096, SC1);
    }
    v4u up[4];
    for (u32 sp = 0; sp < SPIN_CAP; ++sp) {
    CLOB();
      u32 bad = 0;
#pragma unroll
      for (int i = 0; i < 4; ++i) up[i] = ld16c(ws, l16, W_UPD + i * 1024);
#pragma unroll
      for (int i = 0; i < 4; ++i) bad |= tagdiff(up[i], tag);
      if (__all(bad == 0)) break;
    }
    float dl[2], sD = 0.f;
#pragma unroll
    for (int i = 0; i < 4; ++i) sD += lo16(dn[i]) + hi16(dn[i]);
    sD = wave_sum(sD);
#pragma unroll
    for (int t = 0; t < 2; ++t) {
      const float m = t ? s2.m1 : s2.m0, rs = t ? s2.s1 : s2.s0;
      float s = 0.f, q = 0.f, dnew = 0.f, dold = 0.f;
#pragma unroll
      for (int i = 0; i < 4; ++i) {
        const u32 xx = t ? xb[i] : xa[i];
#pragma unroll
        for (int h = 0; h < 2; ++h) {
          const float x1 = h ? hi16(xx) : lo16(xx), dd = h ? hi16(dn[i]) : lo16(dn[i]);
          const float x2 = rbf(x1 + rbf(rbf((x1 - m) * rs) + hi16(up[i][2 * t + h])));
          s += x2; q += x2 * x2; dnew += x2 * dd; dold += x1 * dd;
        }
      }
      s = wave_sum(s); q = wave_sum(q); dnew = wave_sum(dnew); dold = wave_sum(dold);
      const float m2 = s * (1.f / 512), rs2 = __builtin_amdgcn_rsqf(fmaxf(q * (1.f / 512) - m2 * m2, 0.f) + EPS);
      const float var = fmaxf(1.f / (rs * rs) - EPS, 0.f), c3 = (1.f + rs) * __builtin_amdgcn_rsqf(var * (1.f + rs) * (1.f + rs) + EPS);
      dl[t] = ((dnew - m2 * sD) * rs2 - (dold - m * sD) * c3) * (n < 512 ? 1.f : -1.f);
    }
    if (lane == 0) st16c(v4u{asu(dl[0]), asu(dl[1]), tag, tag}, ws, 0, W_DAB + b * 128 + w * 16);
  }
  STAMP(13);
  float res[16];
  {
    float S, Q, Dt;
    gather_stats(ws, W_STAT1 + tb2 * 8192, lane, tag, S, Q, Dt);
    const float mean = S * (1.f / 512), rs = __builtin_amdgcn_rsqf(fmaxf(Q * (1.f / 512) - mean * mean, 0.f) + EPS);
    const auto sm = __builtin_amdgcn_permlane16_swap(asu(mean), asu(mean), false, false), sr = __builtin_amdgcn_permlane16_swap(asu(rs), asu(rs), false, false);
    const rsrc_t rCr = mk(P.Cr, 8 * MB), rCi = mk(P.Ci, 8 * MB);
    float cc[2][4];
#pragma unroll
    for (int mt = 0; mt < 2; ++mt) {
      const int ro = (tok0 + 4 * mt) * 4096;
      cc[mt][0] = lo16(__builtin_amdgcn_raw_buffer_load_b16(rCr, ro, s2.t0 * 2, 0));
      cc[mt][1] = lo16(__builtin_amdgcn_raw_buffer_load_b16(rCr, ro, s2.t1 * 2, 0));
      cc[mt][2] = lo16(__builtin_amdgcn_raw_buffer_load_b16(rCi, ro, s2.t0 * 2, 0));
      cc[mt][3] = lo16(__builtin_amdgcn_raw_buffer_load_b16(rCi, ro, s2.t1 * 2, 0));
    }
    v4u up[4];
#pragma unroll
    for (int i = 0; i < 4; ++i) up[i] = ld16c(ws, g * 64 + i * 16, W_UPD + ec * 256);
    v4u dv;
    for (u32 sp = 0; sp < SPIN_CAP; ++sp) {
    CLOB();
      dv = ld16c(ws, (lane >> 5) * 16384 + ((lane >> 2) & 7) * 128 + (lane & 3) * 16, W_DAB + ec * 1024);
      if (__all(dv.z == tag)) break;
    }
    *(LAS v2u*)(L + L_TR + w * 1024 + lane * 8) = v2u{dv.x, dv.y};
    LGK0();
#pragma unroll
    for (int q = 0; q < 4; ++q) {
      const v4f da = *(const LAS v4f*)(L + L_TR + w * 1024 + g * 64 + q * 16), db = *(const LAS v4f*)(L + L_TR + w * 1024 + 256 + g * 64 + q * 16);
#pragma unroll
      for (int mt = 0; mt < 2; ++mt) {
        const float m = asf(sm[mt]), r = asf(sr[mt]);
        const int t = tok0 + 4 * mt;
#pragma unroll
        for (int h = 0; h < 2; ++h) {
          const float x1 = h ? hi16(x1p[mt * 4 + q]) : lo16(x1p[mt * 4 + q]);
          const float u = t == s2.t0 ? hi16(up[q][h]) : t == s2.t1 ? hi16(up[q][2 + h]) : 0.f;
          const float x2 = rbf(x1 + rbf(rbf((x1 - m) * r) + u));
          res[mt * 8 + q * 2 + h] = x2 + (cc[mt][0] * da[2 * h] + cc[mt][1] * da[2 * h + 1] + cc[mt][2] * db[2 * h] + cc[mt][3] * db[2 * h + 1]);
        }
      }
    }
  }
#pragma unroll
  for (int q = 0; q < 16; ++q) PIN(res[q]);
  fresh();
  STAMP(14);
  wait_flags(ws, W_F3 + fvo(), 4, lane, tag, tagh);
  STAMP(15);
  u32 x3p[8];
  {
    float y[16];
    gemm2(W_ABF2, W_EXC2, y, [&](auto, auto) {});
    STAMP(16);
    fresh();
#pragma unroll
    for (int q = 0; q < 16; ++q) y[q] += res[q];
    tile_out(y, x3p, W_XF3, W_STAT3, true);
  }
  DRAIN();
  STAMP(17);
  v4u o[2];
  {
    float S, Q, Dt;
    gather_stats(ws, W_STAT3 + tb2 * 8192, lane, tag, S, Q, Dt);
    const float mean = S * (1.f / 512), rs = __builtin_amdgcn_rsqf(fmaxf(Q * (1.f / 512) - mean * mean, 0.f) + EPS);
    publish_cand(ws, W_CAND4 + (ec * 16 + p2) * 128 + (tb2 & 3) * 32, lane, tag, tb2, (Dt - mean * Wsum) * rs, mean, rs);
    const auto sm = __builtin_amdgcn_permlane16_swap(asu(mean), asu(mean), false, false), sr = __builtin_amdgcn_permlane16_swap(asu(rs), asu(rs), false, false);
#pragma unroll
    for (int q = 0; q < 8; ++q) {
      const float m = asf(sm[q >> 2]), r = asf(sr[q >> 2]), a0 = lo16(x3p[q]), a1 = hi16(x3p[q]);
      o[q >> 2][q & 3] = pk(a0 + rbf((a0 - m) * r), a1 + rbf((a1 - m) * r));
    }
  }
  STAMP(18);
  DRAIN();
  const Sel s4 = expert_front(W_CAND4, W_XF3, W_PY4);
  STAMP(19);
  const rsrc_t rO = mk(P.out, 2 * MB);
#pragma unroll
  for (int mt = 0; mt < 2; ++mt) {
    const int t = tok0 + 4 * mt;
    if (t != s4.t0 && t != s4.t1) st16(o[mt], rO, t * 1024 + g * 16, ec * 64);
  }
  if (b == 0 && tid == 0) __builtin_amdgcn_raw_buffer_store_b64(v2u{tag, tagh}, ws, 0, W_EPOCH, SC1);
  if (!isGU) {
    const int d = rho * 16 + (lane & 7) * 2, xo = (d >> 5) * 1024 + ((d >> 3) & 3) * 256 + (d & 7) * 2;
    const u32 xa = __builtin_amdgcn_raw_buffer_load_b32(ws, xo, W_XF3 + xf_tok(s4.t0), SC1);
    const u32 xb = __builtin_amdgcn_raw_buffer_load_b32(ws, xo, W_XF3 + xf_tok(s4.t1), SC1);
    float u[4];
    expert_back(W_PY4, s4, u);
    if (tid < 8) {
      const u32 oa = pk(lo16(xa) + rbf(rbf((lo16(xa) - s4.m0) * s4.s0) + u[0]), hi16(xa) + rbf(rbf((hi16(xa) - s4.m0) * s4.s0) + u[1]));
      const u32 ob = pk(lo16(xb) + rbf(rbf((lo16(xb) - s4.m1) * s4.s1) + u[2]), hi16(xb) + rbf(rbf((hi16(xb) - s4.m1) * s4.s1) + u[3]));
      __builtin_amdgcn_raw_buffer_store_b32(oa, rO, d * 2, s4.t0 * 1024, 0);
      __builtin_amdgcn_raw_buffer_store_b32(ob, rO, d * 2, s4.t1 * 1024, 0);
    }
  }
}

extern "C" void kernel_launch(void* const* d_in, const int* in_sizes, int n_in, void* d_out, int out_size, void* d_ws, size_t ws_size,
                              hipStream_t stream) {
  Params p;
  p.x = (const u16*)d_in[0]; p.Cr = (const u16*)d_in[1]; p.Ci = (const u16*)d_in[2]; p.Dr = (const u16*)d_in[3]; p.Di = (const u16*)d_in[4];
  p.wr = (const u16*)d_in[6]; p.wg = (const u16*)d_in[7]; p.wu = (const u16*)d_in[8]; p.wd = (const u16*)d_in[9];
  p.out = (u16*)d_out; p.ws = (char*)d_ws;
  (void)hipFuncSetAttribute((const void*)fnet_mod_kernel, hipFuncAttributeMaxDynamicSharedMemorySize, L_BYTES);
  fnet_mod_kernel<<<256, 256, L_BYTES, stream>>>(p);
}
```

```cpp
#define STAMPS
#define AMP 0
#define AMP0 0
#include <hip/hip_runtime.h>
#include <hip/hip_bf16.h>

typedef unsigned short u16;
typedef unsigned u32;
typedef unsigned long long u64;
typedef u32 v4u __attribute__((ext_vector_type(4)));
typedef u32 v2u __attribute__((ext_vector_type(2)));
typedef float v4f __attribute__((ext_vector_type(4)));
typedef short v4s __attribute__((ext_vector_type(4)));
typedef __bf16 v2bf __attribute__((ext_vector_type(2)));
typedef __amdgpu_buffer_rsrc_t rsrc_t;
#define LAS __attribute__((address_space(3)))
#define INL __device__ __forceinline__

struct Params {
  const u16 *x, *Cr, *Ci, *Dr, *Di, *wr, *wg, *wu, *wd;
  u16* out;
  char* ws;
};

constexpr int KB = 1024, MB = 1 << 20;
constexpr int W_EPOCH = 0, W_F1 = 32 * KB, W_F2 = 64 * KB, W_F3 = 96 * KB, W_CAND2 = 128 * KB, W_CAND4 = 160 * KB, W_DAB = 192 * KB,
              W_UPD = 224 * KB, W_DBG = 512 * KB, W_STAT1 = 1 * MB, W_STAT3 = 1 * MB + 512 * KB, W_PY2 = 2 * MB, W_PY4 = 3 * MB,
              W_XF1 = 4 * MB, W_XF3 = 6 * MB, W_ABF1 = 8 * MB, W_ABF2 = 12 * MB, W_EXC1 = 16 * MB, W_EXC2 = 25 * MB, W_END = 34 * MB;
constexpr int L_D = 0, L_RED = 64 * KB, L_STG = 64 * KB, L_WG = 112 * KB, L_WU = L_WG + 8 * KB + 128, L_WD = 129 * KB, L_MISC = 144 * KB;
constexpr int L_RS = L_MISC;
constexpr int L_TR = L_MISC + 256;
constexpr int L_H = L_TR + 4 * KB;
constexpr int L_X = L_H + 256;
constexpr int L_STAMP = L_X + KB;
constexpr int L_BYTES = L_STAMP + 512;
constexpr u32 SPIN_CAP = 1u << 17;
constexpr float EPS = 1e-5f;
constexpr int SC1 = 16;

template <int N> struct IC { static constexpr int v = N; };
template <int A, int B, class F> INL void sfor(F&& f) {
  if constexpr (A < B) { f(IC<A>{}); sfor<A + 1, B>(f); }
}

INL rsrc_t mk(const void* p, int bytes) { return __builtin_amdgcn_make_buffer_rsrc((void*)p, 0, bytes, 0x00020000); }
INL int rfl(int v) { return __builtin_amdgcn_readfirstlane(v); }
INL float rflf(float v) { return __builtin_bit_cast(float, __builtin_amdgcn_readfirstlane(__builtin_bit_cast(int, v))); }
INL v4u ld16c(rsrc_t r, int vo, int so) { return __builtin_amdgcn_raw_buffer_load_b128(r, vo, so, SC1); }
INL v4u ld16(rsrc_t r, int vo, int so) { return __builtin_amdgcn_raw_buffer_load_b128(r, vo, so, 0); }
INL void st16c(v4u v, rsrc_t r, int vo, int so) { __builtin_amdgcn_raw_buffer_store_b128(v, r, vo, so, SC1); asm volatile("s_nop 1" ::"v"(v) : "memory"); }
INL void st16(v4u v, rsrc_t r, int vo, int so) { __builtin_amdgcn_raw_buffer_store_b128(v, r, vo, so, 0); asm volatile("s_nop 1" ::"v"(v) : "memory"); }
INL float asf(u32 u) { return __builtin_bit_cast(float, u); }
INL u32 asu(float f) { return __builtin_bit_cast(u32, f); }
INL float lo16(u32 u) { return asf(u << 16); }
INL float hi16(u32 u) { return asf(u & 0xffff0000u); }
INL u32 pk(float a, float b) { v2bf p = {(__bf16)a, (__bf16)b}; return __builtin_bit_cast(u32, p); }
INL float rbf(float a) { return lo16(pk(a, a)); }
#define CLOB() asm volatile("" ::: "memory")
#define DRAIN() asm volatile("s_waitcnt vmcnt(0)" ::: "memory")
#define VMW(n) asm volatile("s_waitcnt vmcnt(%0)" ::"i"(n) : "memory")
#define LGK0() asm volatile("s_waitcnt lgkmcnt(0)" ::: "memory")
#define BAR() asm volatile("s_waitcnt lgkmcnt(0)\n\ts_barrier" ::: "memory")
INL void MFMA0(v4f& acc, const v4u& a, const v4u& b) { asm volatile("v_mfma_f32_16x16x32_bf16 %0, %1, %2, 0" : "=&v"(acc) : "v"(a), "v"(b)); }
INL void MFMA(v4f& acc, const v4u& a, const v4u& b) { asm volatile("v_mfma_f32_16x16x32_bf16 %0, %1, %2, %0" : "+v"(acc) : "v"(a), "v"(b)); }
INL void MFMA0A(v4f& acc, const v4u& a, const v4u& b) { asm volatile("v_mfma_f32_16x16x32_bf16 %0, %1, %2, 0" : "=&v"(acc) : "v"(a), "a"(b)); }
INL void MFMAA(v4f& acc, const v4u& a, const v4u& b) { asm volatile("v_mfma_f32_16x16x32_bf16 %0, %1, %2, %0" : "+v"(acc) : "v"(a), "a"(b)); }
template <class T> INL void PIN(T& x) { asm volatile("" : "+v"(x)); }
INL void PINA(v4u& x) { asm volatile("" : "+a"(x)); }
INL void VADD(float& d, float s) { asm volatile("v_add_f32 %0, %0, %1" : "+v"(d) : "v"(s)); }
INL void VADD3(float& d, float a, float b) { asm volatile("v_add_f32 %0, %1, %2" : "=v"(d) : "v"(a), "v"(b)); }

INL void dma_unit(int lds, const int (&vo)[4], rsrc_t r, int so) {
  asm volatile(
      "s_mov_b32 m0, %0\n\ts_nop 0\n\t"
      "buffer_load_dwordx4 %1, %5, %6 offen lds\n\t"
      "buffer_load_dwordx4 %2, %5, %6 offen offset:1024 lds\n\t"
      "buffer_load_dwordx4 %3, %5, %6 offen offset:2048 lds\n\t"
      "buffer_load_dwordx4 %4, %5, %6 offen offset:3072 lds"
      ::"s"(lds), "v"(vo[0]), "v"(vo[1]), "v"(vo[2]), "v"(vo[3]), "s"(r), "s"(so) : "memory");
}
INL void dma_one(int lds, int vo, rsrc_t r, int so) {
  asm volatile("s_mov_b32 m0, %0\n\ts_nop 0\n\tbuffer_load_dwordx4 %1, %2, %3 offen lds" ::"s"(lds), "v"(vo), "s"(r), "s"(so) : "memory");
}

template <int CTRL> INL u32 dppu(u32 v) { return (u32)__builtin_amdgcn_update_dpp(0, (int)v, CTRL, 0xf, 0xf, true); }
template <int CTRL> INL float dppf(float v) { return asf(dppu<CTRL>(asu(v))); }
INL u32 umax(u32 a, u32 b) { return a > b ? a : b; }
INL u32 umin(u32 a, u32 b) { return a < b ? a : b; }
INL u32 row_max(u32 v) { v = umax(v, dppu<0x128>(v)); v = umax(v, dppu<0x124>(v)); v = umax(v, dppu<0x122>(v)); return umax(v, dppu<0x121>(v)); }
INL float row_sum(float v) { v += dppf<0x128>(v); v += dppf<0x124>(v); v += dppf<0x122>(v); return v + dppf<0x121>(v); }
INL u32 max16(u32 v) { auto s = __builtin_amdgcn_permlane16_swap(v, v, false, false); return umax(s[0], s[1]); }
INL u32 max32(u32 v) { auto s = __builtin_amdgcn_permlane32_swap(v, v, false, false); return umax(s[0], s[1]); }
INL float sum16(float v) { auto s = __builtin_amdgcn_permlane16_swap(asu(v), asu(v), false, false); return asf(s[0]) + asf(s[1]); }
INL float sum32(float v) { auto s = __builtin_amdgcn_permlane32_swap(asu(v), asu(v), false, false); return asf(s[0]) + asf(s[1]); }
INL float wave_sum(float v) { return sum32(sum16(row_sum(v))); }
INL float red_g(float a, float b) { auto s = __builtin_amdgcn_permlane16_swap(asu(a), asu(b), false, false); return sum32(asf(s[0]) + asf(s[1])); }

#define STAMP(i) do { if (w == 0) { u64 t_; asm volatile("s_memrealtime %0\n\ts_waitcnt lgkmcnt(0)" : "=s"(t_)::"memory"); if (lane == 0) *(LAS u64*)(L + L_STAMP + 8 * (i)) = t_; } } while (0)

struct Sel { int t0, t1; float m0, m1, s0, s1, r0, r1; };

INL void wait_flags(rsrc_t ws, int vo, int n, int lane, u32 tlo, u32 thi) {
  for (u32 sp = 0; sp < SPIN_CAP; ++sp) {
    CLOB();
    v2u f = {tlo, thi};
    if (lane < n) f = __builtin_amdgcn_raw_buffer_load_b64(ws, vo, 0, SC1);
    if (__all(f.x == tlo && f.y == thi)) break;
  }
  asm volatile("" ::: "memory");
}
INL void gather_stats(rsrc_t ws, int so, int lane, u32 tag, float& S, float& Q, float& Dt) {
  for (u32 sp = 0; sp < SPIN_CAP; ++sp) {
    CLOB();
    v4u v[8];
#pragma unroll
    for (int i = 0; i < 8; ++i) v[i] = ld16c(ws, lane * 16, so + i * 1024);
    bool ok = true;
    S = 0.f; Q = 0.f; Dt = 0.f;
#pragma unroll
    for (int i = 0; i < 8; ++i) { ok &= v[i].w == tag; S += asf(v[i].x); Q += asf(v[i].y); Dt += asf(v[i].z); }
    if (__all(ok)) break;
  }
  S = sum32(S); Q = sum32(Q); Dt = sum32(Dt);
}
INL u32 mkkey(float r, int t) {
  u32 b = pk(r, r) & 0xffffu;
  b = (b & 0x8000u) ? (b ^ 0xffffu) : (b | 0x8000u);
  return (b << 16) | (u32)(2047 - t);
}
INL float key_r(u32 k) { u32 b = k >> 16; b = (b & 0x8000u) ? (b & 0x7fffu) : (b ^ 0xffffu); return asf(b << 16); }
INL void publish_cand(rsrc_t ws, int so, int lane, u32 tag, int tb, float r, float mean, float rs) {
  const int c = lane & 15, mt = (lane >> 4) & 1;
  const u32 key = mkkey(r, 32 * tb + 8 * (c >> 2) + 4 * mt + (c & 3));
  const u32 m1 = max16(row_max(key));
  const u32 m2 = max16(row_max(key == m1 ? 0u : key));
  if (lane < 32 && (key == m1 || key == m2)) st16c(v4u{asu(mean), asu(rs), key, tag}, ws, key == m1 ? 0 : 16, so);
}
INL Sel select_top2(rsrc_t ws, int so, int lane, u32 tag) {
  v4u a, b;
  for (u32 sp = 0; sp < SPIN_CAP; ++sp) {
    CLOB();
    a = ld16c(ws, lane * 16, so);
    b = ld16c(ws, lane * 16, so + 1024);
    if (__all(a.w == tag && b.w == tag)) break;
  }
  const u32 hi = umax(a.z, b.z), lo = umin(a.z, b.z);
  const u32 m1 = max32(max16(row_max(hi)));
  const u32 m2 = max32(max16(row_max(hi == m1 ? lo : hi)));
  const int l1 = __builtin_ctzll(__ballot(hi == m1) | (1ull << 63)), l2 = __builtin_ctzll(__ballot(hi == m2 || lo == m2) | (1ull << 63));
  const u32 mA = a.z == m1 ? a.x : b.x, sA = a.z == m1 ? a.y : b.y, mB = a.z == m2 ? a.x : b.x, sB = a.z == m2 ? a.y : b.y;
  Sel s;
  const u32 k1 = (u32)rfl((int)m1), k2 = (u32)rfl((int)m2);
  s.t0 = 2047 - (int)(k1 & 2047u); s.t1 = 2047 - (int)(k2 & 2047u);
  s.r0 = key_r(k1); s.r1 = key_r(k2);
  s.m0 = asf(__builtin_amdgcn_readlane(mA, l1)); s.s0 = asf(__builtin_amdgcn_readlane(sA, l1));
  s.m1 = asf(__builtin_amdgcn_readlane(mB, l2)); s.s1 = asf(__builtin_amdgcn_readlane(sB, l2));
  return s;
}
INL int xf_tok(int t) { const int r = t & 31; return ((t >> 5) * 2 + ((r >> 2) & 1)) * 16384 + (4 * (r >> 3) + (r & 3)) * 16; }
INL u32 tagb(float v, u32 tag, int i) { return (asu(v) & 0xffffff00u) | ((tag >> (8 * i)) & 0xffu); }
INL u32 tagdiff(v4u v, u32 tag) { return ((v.x ^ tag) | (v.y ^ (tag >> 8)) | (v.z ^ (tag >> 16)) | (v.w ^ (tag >> 24))) & 0xffu; }
INL float sigm(float r) { return rbf(1.f / (1.f + __expf(-r))); }

__global__ void __launch_bounds__(256, 1) fnet_mod_kernel(Params P) {
  extern __shared__ __attribute__((aligned(16))) char smem[];
  LAS char* L = (LAS char*)smem;
  int tid = threadIdx.x, lane = tid & 63;
  const int w = rfl(tid >> 6);
  const int b = blockIdx.x, xcd = b & 7, loc = b >> 3;
  const int k2 = xcd & 3, p2 = (xcd >> 2) * 8 + (loc >> 2), j2 = loc & 3, ec = 4 * j2 + k2;
  const int p1 = 4 * (xcd >> 1) + (loc >> 3), i1 = 8 * (xcd & 1) + (loc & 7);
  const int fline = xcd + 8 * (loc >> 3), cg = fline * 8 + (loc & 7), rho = (xcd - 4) * 8 + (loc & 7);
  const bool isGU = fline < 28;
  int c = lane & 15, g = lane >> 4, l16 = lane * 16, tok0 = 0;
  const int tb2 = 4 * p2 + ((w + j2) & 3);
  auto fresh = [&]() { PIN(l16); lane = l16 >> 4; tid = w * 64 + lane; c = lane & 15; g = lane >> 4; tok0 = 32 * tb2 + 8 * (c >> 2) + (c & 3); };
  const rsrc_t ws = mk(P.ws, W_END);
  auto stamp = [&](int i) { STAMP(i); };
  auto cstamp = [&](int i) { if (w == 0) { u64 t_; asm volatile("s_memtime %0\n\ts_waitcnt lgkmcnt(0)" : "=s"(t_)::"memory"); if (lane == 0) *(LAS u64*)(L + L_STAMP + 8 * (i)) = t_; } };
  STAMP(0);

  int vo1[4], vo4[4];
  {
    const int rowl = 8 * (lane >> 5) + ((lane >> 3) & 3), ch0 = ((lane & 7) ^ (lane >> 4)) * 16;
#pragma unroll
    for (int i = 0; i < 4; ++i) {
      const int row = rowl + 16 * (i & 1) + 4 * (i >> 1), ch = (ch0 ^ ((i & 1) * 64)) + 3072 - 1024 * i;
      vo1[i] = row * 1024 + ch; vo4[i] = row * 4096 + ch;
    }
  }
  const rsrc_t rD = mk((const char*)(i1 < 8 ? P.Dr : P.Di) + (i1 & 7) * 65536 - 3072, 65536 + 3072);
  const rsrc_t rX = mk((const char*)P.x + (128 * p1 + 32 * w) * 1024 - 3072, 32768 + 3072);
  const rsrc_t rC = mk((const char*)((k2 >> 1) ? P.Ci : P.Cr) + (size_t)p2 * 128 * 4096 + (k2 & 1) * 2048 + w * 512 - 3072, 127 * 4096 + 512 + 3072);
  const int stg = L_STG + w * 20480;
  auto dmaD = [&](int r) { const int idx = w + 4 * ((r + p1) & 3); dma_unit(L_D + idx * 4096, vo1, rD, (idx >> 3) * 32768 + (idx & 7) * 128); };
  auto dmaX = [&](int u) { dma_unit(stg + (u % 5) * 4096, vo1, rX, ((u + i1) & 7) * 128); };
  auto dmaC = [&](int n) { dma_unit(stg + ((8 + n) % 5) * 4096, vo4, rC, ((w + j2 + 1 + (n >> 2)) & 3) * 131072 + (n & 3) * 128); };
  asm volatile("s_nop 4" ::: "memory");
  dmaD(0); dmaX(0);
  const v2u ep = __builtin_amdgcn_raw_buffer_load_b64(ws, 0, W_EPOCH, SC1);
  dmaD(1); dmaD(2); dmaD(3);
  dmaX(1); dmaX(2); dmaX(3); dmaX(4);
  int A0 = c * 128 + ((g ^ (c >> 1)) * 16), A1 = A0 ^ 64;
  v4u ones = {0x3f803f80u, 0x3f803f80u, 0x3f803f80u, 0x3f803f80u};
  PIN(ones);
  VMW(16);
  asm volatile("s_barrier" ::: "memory");
  STAMP(1);
  {
    v4f r;
    const int base = L_D + (w >> 1) * 32768 + (w & 1) * 2048;
    sfor<0, 16>([&](auto I) {
      constexpr int i = I.v;
      v4u d = *(const LAS v4u*)(L + base + ((i & 1) ? A1 : A0) + (i >> 1) * 4096);
      if constexpr (i == 0) MFMA0(r, ones, d); else MFMA(r, ones, d);
    });
    asm volatile("s_nop 7" : "+v"(r));
    if (g == 0) *(LAS float*)(L + L_RS + (w * 16 + c) * 4) = r[0];
  }
  v4f acc[8], accS[2], accQ[2];
  sfor<0, 8>([&](auto U) {
    constexpr int u = U.v;
    VMW(16);
    v4u xf[4], df[8];
    const int sl = stg + (u % 5) * 4096, dk = L_D + ((u + i1) & 7) * 4096;
#pragma unroll
    for (int i = 0; i < 4; ++i) xf[i] = *(const LAS v4u*)(L + sl + ((i & 1) ? A1 : A0) + (i >> 1) * 2048);
#pragma unroll
    for (int i = 0; i < 4; ++i) PIN(xf[i]);
    LGK0();
    if constexpr (u + 5 < 8) dmaX(u + 5); else dmaC(u + 5 - 8);
#pragma unroll
    for (int i = 0; i < 8; ++i) df[i] = *(const LAS v4u*)(L + dk + ((i >> 2) ? A1 : A0) + ((i >> 1) & 1) * 32768 + (i & 1) * 2048);
    sfor<0, 2>([&](auto KB_) {
      constexpr int kb = KB_.v;
      sfor<0, 2>([&](auto MT) {
        constexpr int mt = MT.v;
        if constexpr (u == 0 && kb == 0) { MFMA0(accS[mt], xf[mt * 2 + kb], ones); MFMA0(accQ[mt], xf[mt * 2 + kb], xf[mt * 2 + kb]); }
        else { MFMA(accS[mt], xf[mt * 2 + kb], ones); MFMA(accQ[mt], xf[mt * 2 + kb], xf[mt * 2 + kb]); }
      });
      sfor<0, 8>([&](auto I) {
        constexpr int i = I.v, mt = i & 1, nn = i >> 1;
        if constexpr (u == 0 && kb == 0) MFMA0(acc[mt * 4 + nn], xf[mt * 2 + kb], df[kb * 4 + nn]);
        else MFMA(acc[mt * 4 + nn], xf[mt * 2 + kb], df[kb * 4 + nn]);
      });
    });
  });
  asm volatile("s_nop 7" : "+v"(acc[0]), "+v"(acc[1]), "+v"(acc[2]), "+v"(acc[3]), "+v"(acc[4]), "+v"(acc[5]), "+v"(acc[6]), "+v"(acc[7]),
               "+v"(accS[0]), "+v"(accS[1]), "+v"(accQ[0]), "+v"(accQ[1]));
  BAR();
  STAMP(2);
  float RS[4];
#pragma unroll
  for (int i = 0; i < 4; ++i) RS[i] = *(const LAS float*)(L + L_RS + (i * 16 + c) * 4);
  const float sgn = i1 < 8 ? 1.f : -1.f;
  auto gemm1_out = [&](const float (&mean)[8], const float (&sc)[8], int base) {
#pragma unroll
    for (int nn = 0; nn < 4; ++nn) {
      float v[8];
#pragma unroll
      for (int q = 0; q < 8; ++q) v[q] = (acc[(q >> 2) * 4 + nn][q & 3] - mean[q] * RS[nn]) * sc[q];
      st16c(v4u{pk(v[0], v[1]), pk(v[2], v[3]), pk(v[4], v[5]), pk(v[6], v[7])}, ws, l16, base + ((4 * i1 + nn) * 64 + 4 * p1 + w) * 1024);
    }
  };
  {
    float mean[8], sc[8];
#pragma unroll
    for (int q = 0; q < 8; ++q) {
      const float Q = asf(__builtin_amdgcn_ds_bpermute((20 * g + (q & 3)) * 4, asu(accQ[q >> 2][q & 3])));
      mean[q] = accS[q >> 2][q & 3] * (1.f / 512);
      sc[q] = sgn * __builtin_amdgcn_rsqf(fmaxf(Q * (1.f / 512) - mean[q] * mean[q], 0.f) + EPS);
    }
    gemm1_out(mean, sc, W_ABF1);
  }
  const u64 epoch = (((u64)(u32)rfl((int)ep.y) << 32) | (u32)rfl((int)ep.x)) + 1;
  const u32 tag = (u32)epoch, tagh = (u32)(epoch >> 32);
  {
    const v4u z = {tag - 1, tag - 1, tag - 1, tag - 1};
    if (w == 0 && lane < 40) {
      const int i = lane >> 3;
      const int off = i == 0 ? W_F2 + (p2 * 16 + ec) * 128 : i == 1 ? W_F3 + (p1 * 16 + i1) * 128 : i == 2 ? W_CAND2 + (i1 * 16 + p1) * 128
                    : i == 3 ? W_CAND4 + (ec * 16 + p2) * 128 : W_DAB + b * 128;
      st16c(z, ws, off + (lane & 7) * 16, 0);
    }
  }
  STAMP(3);
  v4u cf[64];
  DRAIN();
  asm volatile("s_barrier" ::: "memory");
  if (tid < 8) st16c(v4u{tag, tagh, tag, tagh}, ws, l16, W_F1 + (p1 * 16 + i1) * 128);
  STAMP(4);
  sfor<0, 16>([&](auto N) {
    constexpr int n = N.v;
    if constexpr (n >= 5) VMW(n + 4 < 16 ? 16 : (15 - n) * 4);
    const int sl = stg + ((8 + n) % 5) * 4096;
    sfor<0, 4>([&](auto I) {
      constexpr int i = I.v;
      cf[(n >> 2) * 16 + (i >> 1) * 8 + (n & 3) * 2 + (i & 1)] = *(const LAS v4u*)(L + sl + ((i & 1) ? A1 : A0) + (i >> 1) * 2048);
    });
    sfor<0, 4>([&](auto I) { constexpr int i = I.v; PINA(cf[(n >> 2) * 16 + (i >> 1) * 8 + (n & 3) * 2 + (i & 1)]); });
    LGK0();
    if constexpr (n + 5 < 16) dmaC(n + 5);
  });
  STAMP(5);

  auto fvo = [&]() { return ((8 * (k2 & 1) + 2 * w + ((lane >> 1) & 1)) * 16 + 8 * (k2 >> 1) + 2 * j2 + (lane & 1)) * 128; };

  auto gemm2 = [&](int abf, int exc, float (&y)[16], auto&& hook) {
    int redW[3];
#pragma unroll
    for (int u = 0; u < 3; ++u) redW[u] = L_RED + (((w + 1 + u) & 3) * 3 + u) * 4096 + l16;
    const int redR = L_RED + w * 12288 + l16;
    v4u ab[2][16];
    v4f a[2][4];
    float base[16], bx[16];
    v4u xe[9];
    auto abso = [&](int i, int f) { return abf + (((16 * (k2 >> 1) + 4 * j2 + ((k2 + 1 + i) & 3)) * 2 + (f >> 3)) * 64 + 32 * (k2 & 1) + 8 * w + (f & 7)) * 1024; };
    const int excin = exc + ((p2 * 16 + ec) * 12 + w) * 3072;
#pragma unroll
    for (int f = 0; f < 16; ++f) ab[0][f] = ld16c(ws, l16, abso(0, f));
    sfor<0, 4>([&](auto I) {
      constexpr int i = I.v;
      v4f rd[12];
      u32 pkd[8];
      sfor<0, 128>([&](auto N) {
        constexpr int n = N.v, u = n >> 5, ch = (n >> 3) & 3, ks = n & 7;
        if constexpr (ks == 0) MFMA0A(a[u & 1][ch], ab[i & 1][(ch & 1) * 8 + ks], cf[u * 16 + (ch >> 1) * 8 + ks]);
        else MFMAA(a[u & 1][ch], ab[i & 1][(ch & 1) * 8 + ks], cf[u * 16 + (ch >> 1) * 8 + ks]);
        if constexpr (i > 0 && n >= 8 && n < 24) VADD(base[n - 8], a[1][(n - 8) >> 2][(n - 8) & 3]);
        if constexpr (i > 0 && n >= 18 && n < 26) { pkd[n - 18] = pk(base[2 * (n - 18)], base[2 * (n - 18) + 1]); PIN(pkd[n - 18]); }
        if constexpr (i > 0 && n >= 26 && n < 29) {
          constexpr int q = n - 26;
          st16c(v4u{pkd[3 * q], pkd[3 * q + 1], q < 2 ? pkd[(3 * q + 2) & 7] : tag, tag}, ws, l16,
                exc + (((p2 * 16 + 4 * j2 + ((k2 + i) & 3)) * 3 + (i - 1)) * 4 + w) * 3072 + q * 1024);
        }
        if constexpr (n == 14) BAR();
        if constexpr (n >= 15 && n <= 103 && (n & 7) == 7) {
          constexpr int m = (n - 15) >> 3;
          PIN(a[(m >> 2) & 1][m & 3]);
          *(LAS v4f*)(L + redW[m >> 2] + (m & 3) * 1024) = a[(m >> 2) & 1][m & 3];
        }
        if constexpr (n == 105) BAR();
        if constexpr (n >= 105 && n < 117) rd[n - 105] = *(const LAS v4f*)(L + redR + (((n - 105) % 3) * 4 + (n - 105) / 3) * 1024);
        if constexpr (n >= 112) {
          constexpr int q = n - 112;
          VADD3(base[q], rd[(q >> 2) * 3][q & 3], rd[(q >> 2) * 3 + 1][q & 3]);
          VADD(base[q], rd[(q >> 2) * 3 + 2][q & 3]);
        }
        if constexpr (i < 3 && n >= 30 && n < 94 && (n & 3) == 2) ab[(i + 1) & 1][(n - 30) >> 2] = ld16c(ws, l16, abso(i + 1, (n - 30) >> 2));
        if constexpr (i == 3 && n >= 30 && n < 36) xe[n - 30] = ld16c(ws, l16, excin + ((n - 30) / 3) * 12288 + ((n - 30) % 3) * 1024);
        if constexpr (i == 3 && n >= 72 && n < 75) xe[n - 66] = ld16c(ws, l16, excin + 2 * 12288 + (n - 72) * 1024);
        hook(I, N);
      });
    });
    bool ok = true;
#pragma unroll
    for (int q = 0; q < 9; ++q) ok &= xe[q].w == tag;
    for (u32 sp = 0; !__all(ok) && sp < SPIN_CAP; ++sp) {
      CLOB();
      ok = true;
#pragma unroll
      for (int q = 0; q < 9; ++q) xe[q] = ld16c(ws, l16, excin + (q / 3) * 12288 + (q % 3) * 1024);
#pragma unroll
      for (int q = 0; q < 9; ++q) ok &= xe[q].w == tag;
    }
#pragma unroll
    for (int q = 0; q < 8; ++q) {
      const u32 d0 = xe[q / 3][q % 3], d1 = xe[3 + q / 3][q % 3], d2 = xe[6 + q / 3][q % 3];
      bx[2 * q] = lo16(d0) + lo16(d1) + lo16(d2);
      bx[2 * q + 1] = hi16(d0) + hi16(d1) + hi16(d2);
    }
    asm volatile("s_nop 7" : "+v"(a[1][0]), "+v"(a[1][1]), "+v"(a[1][2]), "+v"(a[1][3]));
#pragma unroll
    for (int q = 0; q < 16; ++q) y[q] = (a[1][q >> 2][q & 3] + base[q]) + bx[q];
  };
  const rsrc_t rW = mk(P.wr, 1024);
  auto tile_out = [&](const float (&v)[16], u32 (&xp)[8], int xf, int stat, bool drain_first) {
    const v4u wq = ld16(rW, g * 16, ec * 64);
#pragma unroll
    for (int q = 0; q < 8; ++q) xp[q] = pk(v[2 * q], v[2 * q + 1]);
    st16c(v4u{xp[0], xp[1], xp[2], xp[3]}, ws, l16, xf + ((tb2 * 2 + 0) * 16 + ec) * 1024);
    st16c(v4u{xp[4], xp[5], xp[6], xp[7]}, ws, l16, xf + ((tb2 * 2 + 1) * 16 + ec) * 1024);
    float s[2], q2[2], d[2];
#pragma unroll
    for (int mt = 0; mt < 2; ++mt) {
      s[mt] = 0.f; q2[mt] = 0.f; d[mt] = 0.f;
#pragma unroll
      for (int q = 0; q < 4; ++q) {
        const float a0 = lo16(xp[mt * 4 + q]), a1 = hi16(xp[mt * 4 + q]);
        s[mt] += a0 + a1; q2[mt] += a0 * a0 + a1 * a1; d[mt] += a0 * lo16(wq[q]) + a1 * hi16(wq[q]);
      }
    }
    const float S = red_g(s[0], s[1]), Q = red_g(q2[0], q2[1]), Dt = red_g(d[0], d[1]);
    if (drain_first) DRAIN();
    if (lane < 32) st16c(v4u{asu(S), asu(Q), asu(Dt), tag}, ws, l16, stat + (tb2 * 16 + ec) * 512);
  };

  fresh();
  wait_flags(ws, W_F1 + fvo(), 4, lane, tag, tagh);
  STAMP(6);
  u32 x1p[8];
  {
    float y[16];
    v4u xr[2];
    const rsrc_t rXa = mk(P.x, 2 * MB);
    const rsrc_t rG = mk((const char*)((w >> 1) ? P.wu : P.wg) + cg * 16, isGU ? 512 * 3584 - cg * 16 : 0);
    const rsrc_t rWd = mk((const char*)P.wd + cg * 8192, isGU ? 8192 : 0);
    const int pos = lane & 31;
    const int gvo = (32 * (lane >> 5) + 8 * ((pos >> 2) & 3) + 4 * (pos >> 4) + (pos & 3)) * 3584;
    gemm2(W_ABF1, W_EXC1, y, [&](auto I, auto N) {
      constexpr int i = I.v, n = N.v;
      if constexpr (i == 3 && (n == 40 || n == 41)) xr[n - 40] = ld16(rXa, (tok0 + 4 * (n - 40)) * 1024 + g * 16, ec * 64);
      if constexpr ((i == 1 || i == 2) && (n == 96 || n == 100)) {
        constexpr int q = (i - 1) * 2 + (n - 96) / 4;
        dma_one(((w >> 1) ? L_WU : L_WG) + ((w & 1) * 4 + q) * 1024, gvo, rG, ((w & 1) * 4 + q) * 64 * 3584);
      }
      if constexpr ((i == 1 || i == 2) && n == 98) dma_one(L_WD + (w * 2 + i - 1) * 1024, l16, rWd, (w * 2 + i - 1) * 1024);
    });
    STAMP(7);
    fresh();
#pragma unroll
    for (int q = 0; q < 8; ++q) { y[2 * q] += lo16(xr[q >> 2][q & 3]); y[2 * q + 1] += hi16(xr[q >> 2][q & 3]); }
    tile_out(y, x1p, W_XF1, W_STAT1, false);
  }
  DRAIN();
  asm volatile("s_barrier" ::: "memory");
  if (tid < 8) st16c(v4u{tag, tagh, tag, tagh}, ws, l16, W_F2 + (p2 * 16 + ec) * 128);
  STAMP(8);
  float Wsum;
  {
    const v4u wq = ld16(rW, l16, 0);
    float s = 0.f;
#pragma unroll
    for (int q = 0; q < 4; ++q) s += lo16(wq[q]) + hi16(wq[q]);
    Wsum = rflf(wave_sum(s));
  }
  auto wfrag = [&](int kb) {
    const int trA = ((c >> 1) & 1 ? L_WU : L_WG) + (4 * g + (c >> 2)) * 16 + (c & 1) * 8;
    const v4s lo = __builtin_amdgcn_ds_read_tr16_b64_v4i16((LAS v4s*)(L + trA + kb * 512));
    const v4s hi = __builtin_amdgcn_ds_read_tr16_b64_v4i16((LAS v4s*)(L + trA + kb * 512 + 256));
    const v2u a = __builtin_bit_cast(v2u, lo), bb = __builtin_bit_cast(v2u, hi);
    return v4u{a.x, a.y, bb.x, bb.y};
  };
  auto expert_front = [&](int cand, int xf, int py) {
    if (w == 0) {
      const Sel s = select_top2(ws, cand + (b & 15) * 2048, lane, tag);
      if (isGU) {
        v4u xr[16];
        const int vo = ((c & 1) ? xf_tok(s.t1) : xf_tok(s.t0)) + g * 256;
#pragma unroll
        for (int kb = 0; kb < 16; ++kb) xr[kb] = ld16c(ws, vo, xf + kb * 1024);
        v4f h, cs;
        ones = v4u{0x3f803f80u, 0x3f803f80u, 0x3f803f80u, 0x3f803f80u};
        PIN(ones);
        sfor<0, 16>([&](auto K) {
          constexpr int kb = K.v;
          v4u wf = wfrag(kb);
          asm volatile("s_nop 1" : "+v"(wf));
          if constexpr (kb == 0) { MFMA0(cs, wf, ones); MFMA0(h, wf, xr[kb]); } else { MFMA(cs, wf, ones); MFMA(h, wf, xr[kb]); }
        });
        asm volatile("s_nop 7" : "+v"(h), "+v"(cs));
        const float mean = (c & 1) ? s.m1 : s.m0, rs = (c & 1) ? s.s1 : s.s0;
        v4f o;
#pragma unroll
        for (int i = 0; i < 4; ++i) {
          const float v = rbf((h[i] - mean * cs[i]) * rs);
          const auto sw = __builtin_amdgcn_permlane32_swap(asu(v), asu(v), false, false);
          const float gt = asf(sw[0]), up = asf(sw[1]);
          o[i] = rbf(rbf(gt / (1.f + __expf(-gt))) * up);
        }
        if (c < 2 && g < 2) *(LAS v4f*)(L + L_H + c * 32 + g * 16) = o;
      }
      if (lane == 0) {
        *(LAS v4u*)(L + L_H + 64) = v4u{(u32)s.t0, (u32)s.t1, asu(s.m0), asu(s.m1)};
        *(LAS v4u*)(L + L_H + 80) = v4u{asu(s.s0), asu(s.s1), asu(s.r0), asu(s.r1)};
      }
    }
    u32 wd[8];
#pragma unroll
    for (int i = 0; i < 8; ++i) wd[i] = *(const LAS u32*)(L + L_WD + i * 1024 + tid * 4);
    BAR();
    if (isGU) {
      float y[4] = {0.f, 0.f, 0.f, 0.f};
#pragma unroll
      for (int q = 0; q < 4; ++q) {
        const v4f h = *(const LAS v4f*)(L + L_H + q * 16);
#pragma unroll
        for (int i = 0; i < 4; ++i) { y[(q >> 1) * 2] += h[i] * lo16(wd[(q & 1) * 4 + i]); y[(q >> 1) * 2 + 1] += h[i] * hi16(wd[(q & 1) * 4 + i]); }
      }
      st16c(v4u{tagb(y[0], tag, 0), tagb(y[1], tag, 1), tagb(y[2], tag, 2), tagb(y[3], tag, 3)}, ws, tid * 16, py + cg * 4096);
    }
    const v4u s0 = *(const LAS v4u*)(L + L_H + 64), s1 = *(const LAS v4u*)(L + L_H + 80);
    Sel s;
    s.t0 = rfl((int)s0.x); s.t1 = rfl((int)s0.y); s.m0 = rflf(asf(s0.z)); s.m1 = rflf(asf(s0.w));
    s.s0 = rflf(asf(s1.x)); s.s1 = rflf(asf(s1.y)); s.r0 = rflf(asf(s1.z)); s.r1 = rflf(asf(s1.w));
    return s;
  };
  auto expert_back = [&](int py, const Sel& s, float (&u)[4]) {
    v4f t;
    for (u32 sp = 0; sp < SPIN_CAP; ++sp) {
    CLOB();
      v4u v[7];
#pragma unroll
      for (int i = 0; i < 7; ++i) v[i] = ld16c(ws, (tid >> 3) * 4096 + (tid & 7) * 16, py + rho * 128 + i * 131072);
      u32 bad = 0;
      t = v4f{0.f, 0.f, 0.f, 0.f};
#pragma unroll
      for (int i = 0; i < 7; ++i) { bad |= tagdiff(v[i], tag); t += __builtin_bit_cast(v4f, v[i] & 0xffffff00u); }
      if (__all(bad == 0)) break;
    }
#pragma unroll
    for (int i = 0; i < 4; ++i) t[i] = sum32(sum16(t[i] + dppf<0x128>(t[i])));
    if (lane < 8) *(LAS v4f*)(L + L_X + w * 128 + l16) = t;
    BAR();
    t = v4f{0.f, 0.f, 0.f, 0.f};
#pragma unroll
    for (int i = 0; i < 4; ++i) t += *(const LAS v4f*)(L + L_X + i * 128 + (lane & 7) * 16);
    const float g0 = sigm(s.r0), g1 = sigm(s.r1);
    u[0] = rbf(rbf(t[0]) * g0); u[1] = rbf(rbf(t[1]) * g0); u[2] = rbf(rbf(t[2]) * g1); u[3] = rbf(rbf(t[3]) * g1);
  };

  DRAIN();
  if (w == 0) wait_flags(ws, W_F2 + (p1 * 16 + c) * 128, 16, lane, tag, tagh);
  asm volatile("s_barrier" ::: "memory");
  STAMP(9);
  {
    const int tb1 = 4 * p1 + w;
    A0 = c * 128 + ((g ^ (c >> 1)) * 16); A1 = A0 ^ 64;
#pragma unroll
    for (int i = 0; i < 4; ++i) RS[i] = *(const LAS float*)(L + L_RS + (i * 16 + c) * 4);
    v4u sv[8], xf[32];
#pragma unroll
    for (int i = 0; i < 8; ++i) sv[i] = ld16c(ws, l16, W_STAT1 + tb1 * 8192 + i * 1024);
#pragma unroll
    for (int i = 0; i < 32; ++i) xf[i] = ld16c(ws, l16, W_XF1 + ((tb1 * 2 + ((i >> 1) & 1)) * 16 + 2 * (((i >> 2) + i1) & 7) + (i & 1)) * 1024);
    __builtin_amdgcn_sched_barrier(0);
    float S = 0.f, Q = 0.f, Dt = 0.f;
#pragma unroll
    for (int i = 0; i < 8; ++i) { S += asf(sv[i].x); Q += asf(sv[i].y); Dt += asf(sv[i].z); }
    S = sum32(S); Q = sum32(Q); Dt = sum32(Dt);
    const float mean = S * (1.f / 512), var = fmaxf(Q * (1.f / 512) - mean * mean, 0.f), rs = __builtin_amdgcn_rsqf(var + EPS);
    publish_cand(ws, W_CAND2 + (i1 * 16 + p1) * 128 + w * 32, lane, tag, tb1, (Dt - mean * Wsum) * rs, mean, rs);
    const float c3 = (1.f + rs) * __builtin_amdgcn_rsqf(var * (1.f + rs) * (1.f + rs) + EPS);
    if (lane < 32) { *(LAS float*)(L + L_TR + w * 1024 + lane * 4) = mean; *(LAS float*)(L + L_TR + w * 1024 + 128 + lane * 4) = c3 * sgn; }
    sfor<0, 8>([&](auto U) {
      constexpr int u = U.v;
      v4u df[8];
      const int dk = L_D + ((u + i1) & 7) * 4096;
#pragma unroll
      for (int i = 0; i < 8; ++i) df[i] = *(const LAS v4u*)(L + dk + ((i >> 2) ? A1 : A0) + ((i >> 1) & 1) * 32768 + (i & 1) * 2048);
      sfor<0, 16>([&](auto I) {
        constexpr int i = I.v, kb = i >> 3, mt = i & 1, nn = (i >> 1) & 3;
        if constexpr (u == 0 && kb == 0) MFMA0(acc[mt * 4 + nn], xf[u * 4 + mt * 2 + kb], df[kb * 4 + nn]);
        else MFMA(acc[mt * 4 + nn], xf[u * 4 + mt * 2 + kb], df[kb * 4 + nn]);
      });
    });
    float mn[8], sc[8];
#pragma unroll
    for (int mt = 0; mt < 2; ++mt) {
      const v4f a = *(const LAS v4f*)(L + L_TR + w * 1024 + mt * 64 + g * 16), bb = *(const LAS v4f*)(L + L_TR + w * 1024 + 128 + mt * 64 + g * 16);
#pragma unroll
      for (int i = 0; i < 4; ++i) { mn[mt * 4 + i] = a[i]; sc[mt * 4 + i] = bb[i]; }
    }
    asm volatile("s_nop 7" : "+v"(acc[0]), "+v"(acc[1]), "+v"(acc[2]), "+v"(acc[3]), "+v"(acc[4]), "+v"(acc[5]), "+v"(acc[6]), "+v"(acc[7]));
    STAMP(10);
    gemm1_out(mn, sc, W_ABF2);
  }
  DRAIN();
  asm volatile("s_barrier" ::: "memory");
  if (tid < 8) st16c(v4u{tag, tagh, tag, tagh}, ws, l16, W_F3 + (p1 * 16 + i1) * 128);
  STAMP(11);

  const Sel s2 = expert_front(W_CAND2, W_XF1, W_PY2);
  STAMP(12);
  if (!isGU) {
    float u[4];
    expert_back(W_PY2, s2, u);
    if (tid < 8) st16c(v4u{tagb(u[0], tag, 0), tagb(u[1], tag, 1), tagb(u[2], tag, 2), tagb(u[3], tag, 3)}, ws, l16, W_UPD + rho * 128);
  }
  {
    const int n = 4 * b + w;
    const rsrc_t rDn = mk((const char*)(n < 512 ? P.Dr : P.Di) + (n & 511) * 1024, 1024);
    u32 dn[4], xa[4], xb[4];
    const int xo = (lane >> 4) * 1024 + ((lane >> 2) & 3) * 256 + (lane & 3) * 4;
#pragma unroll
    for (int i = 0; i < 4; ++i) {
      dn[i] = __builtin_amdgcn_raw_buffer_load_b32(rDn, lane * 4, i * 256, 0);
      xa[i] = __builtin_amdgcn_raw_buffer_load_b32(ws, xo, W_XF1 + xf_tok(s2.t0) + i * 4096, SC1);
      xb[i] = __builtin_amdgcn_raw_buffer_load_b32(ws, xo, W_XF1 + xf_tok(s2.t1) + i * 4096, SC1);
    }
    v4u up[4];
    for (u32 sp = 0; sp < SPIN_CAP; ++sp) {
    CLOB();
      u32 bad = 0;
#pragma unroll
      for (int i = 0; i < 4; ++i) up[i] = ld16c(ws, l16, W_UPD + i * 1024);
#pragma unroll
      for (int i = 0; i < 4; ++i) bad |= tagdiff(up[i], tag);
      if (__all(bad == 0)) break;
    }
    float dl[2], sD = 0.f;
#pragma unroll
    for (int i = 0; i < 4; ++i) sD += lo16(dn[i]) + hi16(dn[i]);
    sD = wave_sum(sD);
#pragma unroll
    for (int t = 0; t < 2; ++t) {
      const float m = t ? s2.m1 : s2.m0, rs = t ? s2.s1 : s2.s0;
      float s = 0.f, q = 0.f, dnew = 0.f, dold = 0.f;
#pragma unroll
      for (int i = 0; i < 4; ++i) {
        const u32 xx = t ? xb[i] : xa[i];
#pragma unroll
        for (int h = 0; h < 2; ++h) {
          const float x1 = h ? hi16(xx) : lo16(xx), dd = h ? hi16(dn[i]) : lo16(dn[i]);
          const float x2 = rbf(x1 + rbf(rbf((x1 - m) * rs) + hi16(up[i][2 * t + h])));
          s += x2; q += x2 * x2; dnew += x2 * dd; dold += x1 * dd;
        }
      }
      s = wave_sum(s); q = wave_sum(q); dnew = wave_sum(dnew); dold = wave_sum(dold);
      const float m2 = s * (1.f / 512), rs2 = __builtin_amdgcn_rsqf(fmaxf(q * (1.f / 512) - m2 * m2, 0.f) + EPS);
      const float var = fmaxf(1.f / (rs * rs) - EPS, 0.f), c3 = (1.f + rs) * __builtin_amdgcn_rsqf(var * (1.f + rs) * (1.f + rs) + EPS);
      dl[t] = ((dnew - m2 * sD) * rs2 - (dold - m * sD) * c3) * (n < 512 ? 1.f : -1.f);
    }
    if (lane == 0) st16c(v4u{asu(dl[0]), asu(dl[1]), tag, tag}, ws, 0, W_DAB + b * 128 + w * 16);
  }
  STAMP(13);
  float res[16];
  {
    float S, Q, Dt;
    gather_stats(ws, W_STAT1 + tb2 * 8192, lane, tag, S, Q, Dt);
    const float mean = S * (1.f / 512), rs = __builtin_amdgcn_rsqf(fmaxf(Q * (1.f / 512) - mean * mean, 0.f) + EPS);
    const auto sm = __builtin_amdgcn_permlane16_swap(asu(mean), asu(mean), false, false), sr = __builtin_amdgcn_permlane16_swap(asu(rs), asu(rs), false, false);
    const rsrc_t rCr = mk(P.Cr, 8 * MB), rCi = mk(P.Ci, 8 * MB);
    float cc[2][4];
#pragma unroll
    for (int mt = 0; mt < 2; ++mt) {
      const int ro = (tok0 + 4 * mt) * 4096;
      cc[mt][0] = lo16(__builtin_amdgcn_raw_buffer_load_b16(rCr, ro, s2.t0 * 2, 0));
      cc[mt][1] = lo16(__builtin_amdgcn_raw_buffer_load_b16(rCr, ro, s2.t1 * 2, 0));
      cc[mt][2] = lo16(__builtin_amdgcn_raw_buffer_load_b16(rCi, ro, s2.t0 * 2, 0));
      cc[mt][3] = lo16(__builtin_amdgcn_raw_buffer_load_b16(rCi, ro, s2.t1 * 2, 0));
    }
    v4u up[4];
#pragma unroll
    for (int i = 0; i < 4; ++i) up[i] = ld16c(ws, g * 64 + i * 16, W_UPD + ec * 256);
    v4u dv;
    for (u32 sp = 0; sp < SPIN_CAP; ++sp) {
    CLOB();
      dv = ld16c(ws, (lane >> 5) * 16384 + ((lane >> 2) & 7) * 128 + (lane & 3) * 16, W_DAB + ec * 1024);
      if (__all(dv.z == tag)) break;
    }
    *(LAS v2u*)(L + L_TR + w * 1024 + lane * 8) = v2u{dv.x, dv.y};
    LGK0();
#pragma unroll
    for (int q = 0; q < 4; ++q) {
      const v4f da = *(const LAS v4f*)(L + L_TR + w * 1024 + g * 64 + q * 16), db = *(const LAS v4f*)(L + L_TR + w * 1024 + 256 + g * 64 + q * 16);
#pragma unroll
      for (int mt = 0; mt < 2; ++mt) {
        const float m = asf(sm[mt]), r = asf(sr[mt]);
        const int t = tok0 + 4 * mt;
#pragma unroll
        for (int h = 0; h < 2; ++h) {
          const float x1 = h ? hi16(x1p[mt * 4 + q]) : lo16(x1p[mt * 4 + q]);
          const float u = t == s2.t0 ? hi16(up[q][h]) : t == s2.t1 ? hi16(up[q][2 + h]) : 0.f;
          const float x2 = rbf(x1 + rbf(rbf((x1 - m) * r) + u));
          res[mt * 8 + q * 2 + h] = x2 + (cc[mt][0] * da[2 * h] + cc[mt][1] * da[2 * h + 1] + cc[mt][2] * db[2 * h] + cc[mt][3] * db[2 * h + 1]);
        }
      }
    }
  }
#pragma unroll
  for (int q = 0; q < 16; ++q) PIN(res[q]);
  fresh();
  STAMP(14);
  wait_flags(ws, W_F3 + fvo(), 4, lane, tag, tagh);
  STAMP(15);
  u32 x3p[8];
  {
    float y[16];
    gemm2(W_ABF2, W_EXC2, y, [&](auto I, auto N) {
      if constexpr (N.v == 15) stamp(21 + 2 * I.v);
      if constexpr (N.v == 104) stamp(22 + 2 * I.v);
      if constexpr (N.v == 15 && I.v == 0) cstamp(29);
      if constexpr (N.v == 104 && I.v == 3) cstamp(30);
    });
    STAMP(16);
    fresh();
#pragma unroll
    for (int q = 0; q < 16; ++q) y[q] += res[q];
    tile_out(y, x3p, W_XF3, W_STAT3, true);
  }
  DRAIN();
  STAMP(17);
  v4u o[2];
  {
    float S, Q, Dt;
    gather_stats(ws, W_STAT3 + tb2 * 8192, lane, tag, S, Q, Dt);
    const float mean = S * (1.f / 512), rs = __builtin_amdgcn_rsqf(fmaxf(Q * (1.f / 512) - mean * mean, 0.f) + EPS);
    publish_cand(ws, W_CAND4 + (ec * 16 + p2) * 128 + (tb2 & 3) * 32, lane, tag, tb2, (Dt - mean * Wsum) * rs, mean, rs);
    const auto sm = __builtin_amdgcn_permlane16_swap(asu(mean), asu(mean), false, false), sr = __builtin_amdgcn_permlane16_swap(asu(rs), asu(rs), false, false);
#pragma unroll
    for (int q = 0; q < 8; ++q) {
      const float m = asf(sm[q >> 2]), r = asf(sr[q >> 2]), a0 = lo16(x3p[q]), a1 = hi16(x3p[q]);
      o[q >> 2][q & 3] = pk(a0 + rbf((a0 - m) * r), a1 + rbf((a1 - m) * r));
    }
  }
  STAMP(18);
  DRAIN();
  const Sel s4 = expert_front(W_CAND4, W_XF3, W_PY4);
  STAMP(19);
  const rsrc_t rO = mk(P.out, 2 * MB);
#pragma unroll
  for (int mt = 0; mt < 2; ++mt) {
    const int t = tok0 + 4 * mt;
    if (t != s4.t0 && t != s4.t1) st16(o[mt], rO, t * 1024 + g * 16, ec * 64);
  }
  if (b == 0 && tid == 0) __builtin_amdgcn_raw_buffer_store_b64(v2u{tag, tagh}, ws, 0, W_EPOCH, SC1);
  if (!isGU) {
    const int d = rho * 16 + (lane & 7) * 2, xo = (d >> 5) * 1024 + ((d >> 3) & 3) * 256 + (d & 7) * 2;
    const u32 xa = __builtin_amdgcn_raw_buffer_load_b32(ws, xo, W_XF3 + xf_tok(s4.t0), SC1);
    const u32 xb = __builtin_amdgcn_raw_buffer_load_b32(ws, xo, W_XF3 + xf_tok(s4.t1), SC1);
    float u[4];
    expert_back(W_PY4, s4, u);
    if (tid < 8) {
      const u32 oa = pk(lo16(xa) + rbf(rbf((lo16(xa) - s4.m0) * s4.s0) + u[0]), hi16(xa) + rbf(rbf((hi16(xa) - s4.m0) * s4.s0) + u[1]));
      const u32 ob = pk(lo16(xb) + rbf(rbf((lo16(xb) - s4.m1) * s4.s1) + u[2]), hi16(xb) + rbf(rbf((hi16(xb) - s4.m1) * s4.s1) + u[3]));
      __builtin_amdgcn_raw_buffer_store_b32(oa, rO, d * 2, s4.t0 * 1024, 0);
      __builtin_amdgcn_raw_buffer_store_b32(ob, rO, d * 2, s4.t1 * 1024, 0);
    }
  }
  DRAIN();
  STAMP(20);
  if (w == 0 && b == 100) {
    const u64 ta = *(LAS u64*)(L + L_STAMP + 8 * AMP0), tb = *(LAS u64*)(L + L_STAMP + 8 * AMP);
    const u64 end = __builtin_amdgcn_s_memrealtime() + (tb - ta) * 20;
    while (__builtin_amdgcn_s_memrealtime() < end) __builtin_amdgcn_s_sleep(2);
  }
}

extern "C" void kernel_launch(void* const* d_in, const int* in_sizes, int n_in, void* d_out, int out_size, void* d_ws, size_t ws_size,
                              hipStream_t stream) {
  Params p;
  p.x = (const u16*)d_in[0]; p.Cr = (const u16*)d_in[1]; p.Ci = (const u16*)d_in[2]; p.Dr = (const u16*)d_in[3]; p.Di = (const u16*)d_in[4];
  p.wr = (const u16*)d_in[6]; p.wg = (const u16*)d_in[7]; p.wu = (const u16*)d_in[8]; p.wd = (const u16*)d_in[9];
  p.out = (u16*)d_out; p.ws = (char*)d_ws;
  (void)hipFuncSetAttribute((const void*)fnet_mod_kernel, hipFuncAttributeMaxDynamicSharedMemorySize, L_BYTES);
  fnet_mod_kernel<<<256, 256, L_BYTES, stream>>>(p);
}
```

```cpp
#define STAMPS
#define AMP 29
#define AMP0 0
#include <hip/hip_runtime.h>
#include <hip/hip_bf16.h>

typedef unsigned short u16;
typedef unsigned u32;
typedef unsigned long long u64;
typedef u32 v4u __attribute__((ext_vector_type(4)));
typedef u32 v2u __attribute__((ext_vector_type(2)));
typedef float v4f __attribute__((ext_vector_type(4)));
typedef short v4s __attribute__((ext_vector_type(4)));
typedef __bf16 v2bf __attribute__((ext_vector_type(2)));
typedef __amdgpu_buffer_rsrc_t rsrc_t;
#define LAS __attribute__((address_space(3)))
#define INL __device__ __forceinline__

struct Params {
  const u16 *x, *Cr, *Ci, *Dr, *Di, *wr, *wg, *wu, *wd;
  u16* out;
  char* ws;
};

constexpr int KB = 1024, MB = 1 << 20;
constexpr int W_EPOCH = 0, W_F1 = 32 * KB, W_F2 = 64 * KB, W_F3 = 96 * KB, W_CAND2 = 128 * KB, W_CAND4 = 160 * KB, W_DAB = 192 * KB,
              W_UPD = 224 * KB, W_DBG = 512 * KB, W_STAT1 = 1 * MB, W_STAT3 = 1 * MB + 512 * KB, W_PY2 = 2 * MB, W_PY4 = 3 * MB,
              W_XF1 = 4 * MB, W_XF3 = 6 * MB, W_ABF1 = 8 * MB, W_ABF2 = 12 * MB, W_EXC1 = 16 * MB, W_EXC2 = 25 * MB, W_END = 34 * MB;
constexpr int L_D = 0, L_RED = 64 * KB, L_STG = 64 * KB, L_WG = 112 * KB, L_WU = L_WG + 8 * KB + 128, L_WD = 129 * KB, L_MISC = 144 * KB;
constexpr int L_RS = L_MISC;
constexpr int L_TR = L_MISC + 256;
constexpr int L_H = L_TR + 4 * KB;
constexpr int L_X = L_H + 256;
constexpr int L_STAMP = L_X + KB;
constexpr int L_BYTES = L_STAMP + 512;
constexpr u32 SPIN_CAP = 1u << 17;
constexpr float EPS = 1e-5f;
constexpr int SC1 = 16;

template <int N> struct IC { static constexpr int v = N; };
template <int A, int B, class F> INL void sfor(F&& f) {
  if constexpr (A < B) { f(IC<A>{}); sfor<A + 1, B>(f); }
}

INL rsrc_t mk(const void* p, int bytes) { return __builtin_amdgcn_make_buffer_rsrc((void*)p, 0, bytes, 0x00020000); }
INL int rfl(int v) { return __builtin_amdgcn_readfirstlane(v); }
INL float rflf(float v) { return __builtin_bit_cast(float, __builtin_amdgcn_readfirstlane(__builtin_bit_cast(int, v))); }
INL v4u ld16c(rsrc_t r, int vo, int so) { return __builtin_amdgcn_raw_buffer_load_b128(r, vo, so, SC1); }
INL v4u ld16(rsrc_t r, int vo, int so) { return __builtin_amdgcn_raw_buffer_load_b128(r, vo, so, 0); }
INL void st16c(v4u v, rsrc_t r, int vo, int so) { __builtin_amdgcn_raw_buffer_store_b128(v, r, vo, so, SC1); asm volatile("s_nop 1" ::"v"(v) : "memory"); }
INL void st16(v4u v, rsrc_t r, int vo, int so) { __builtin_amdgcn_raw_buffer_store_b128(v, r, vo, so, 0); asm volatile("s_nop 1" ::"v"(v) : "memory"); }
INL float asf(u32 u) { return __builtin_bit_cast(float, u); }
INL u32 asu(float f) { return __builtin_bit_cast(u32, f); }
INL float lo16(u32 u) { return asf(u << 16); }
INL float hi16(u32 u) { return asf(u & 0xffff0000u); }
INL u32 pk(float a, float b) { v2bf p = {(__bf16)a, (__bf16)b}; return __builtin_bit_cast(u32, p); }
INL float rbf(float a) { return lo16(pk(a, a)); }
#define CLOB() asm volatile("" ::: "memory")
#define DRAIN() asm volatile("s_waitcnt vmcnt(0)" ::: "memory")
#define VMW(n) asm volatile("s_waitcnt vmcnt(%0)" ::"i"(n) : "memory")
#define LGK0() asm volatile("s_waitcnt lgkmcnt(0)" ::: "memory")
#define BAR() asm volatile("s_waitcnt lgkmcnt(0)\n\ts_barrier" ::: "memory")
INL void MFMA0(v4f& acc, const v4u& a, const v4u& b) { asm volatile("v_mfma_f32_16x16x32_bf16 %0, %1, %2, 0" : "=&v"(acc) : "v"(a), "v"(b)); }
INL void MFMA(v4f& acc, const v4u& a, const v4u& b) { asm volatile("v_mfma_f32_16x16x32_bf16 %0, %1, %2, %0" : "+v"(acc) : "v"(a), "v"(b)); }
INL void MFMA0A(v4f& acc, const v4u& a, const v4u& b) { asm volatile("v_mfma_f32_16x16x32_bf16 %0, %1, %2, 0" : "=&v"(acc) : "v"(a), "a"(b)); }
INL void MFMAA(v4f& acc, const v4u& a, const v4u& b) { asm volatile("v_mfma_f32_16x16x32_bf16 %0, %1, %2, %0" : "+v"(acc) : "v"(a), "a"(b)); }
template <class T> INL void PIN(T& x) { asm volatile("" : "+v"(x)); }
INL void PINA(v4u& x) { asm volatile("" : "+a"(x)); }
INL void VADD(float& d, float s) { asm volatile("v_add_f32 %0, %0, %1" : "+v"(d) : "v"(s)); }
INL void VADD3(float& d, float a, float b) { asm volatile("v_add_f32 %0, %1, %2" : "=v"(d) : "v"(a), "v"(b)); }

INL void dma_unit(int lds, const int (&vo)[4], rsrc_t r, int so) {
  asm volatile(
      "s_mov_b32 m0, %0\n\ts_nop 0\n\t"
      "buffer_load_dwordx4 %1, %5, %6 offen lds\n\t"
      "buffer_load_dwordx4 %2, %5, %6 offen offset:1024 lds\n\t"
      "buffer_load_dwordx4 %3, %5, %6 offen offset:2048 lds\n\t"
      "buffer_load_dwordx4 %4, %5, %6 offen offset:3072 lds"
      ::"s"(lds), "v"(vo[0]), "v"(vo[1]), "v"(vo[2]), "v"(vo[3]), "s"(r), "s"(so) : "memory");
}
INL void dma_one(int lds, int vo, rsrc_t r, int so) {
  asm volatile("s_mov_b32 m0, %0\n\ts_nop 0\n\tbuffer_load_dwordx4 %1, %2, %3 offen lds" ::"s"(lds), "v"(vo), "s"(r), "s"(so) : "memory");
}

template <int CTRL> INL u32 dppu(u32 v) { return (u32)__builtin_amdgcn_update_dpp(0, (int)v, CTRL, 0xf, 0xf, true); }
template <int CTRL> INL float dppf(float v) { return asf(dppu<CTRL>(asu(v))); }
INL u32 umax(u32 a, u32 b) { return a > b ? a : b; }
INL u32 umin(u32 a, u32 b) { return a < b ? a : b; }
INL u32 row_max(u32 v) { v = umax(v, dppu<0x128>(v)); v = umax(v, dppu<0x124>(v)); v = umax(v, dppu<0x122>(v)); return umax(v, dppu<0x121>(v)); }
INL float row_sum(float v) { v += dppf<0x128>(v); v += dppf<0x124>(v); v += dppf<0x122>(v); return v + dppf<0x121>(v); }
INL u32 max16(u32 v) { auto s = __builtin_amdgcn_permlane16_swap(v, v, false, false); return umax(s[0], s[1]); }
INL u32 max32(u32 v) { auto s = __builtin_amdgcn_permlane32_swap(v, v, false, false); return umax(s[0], s[1]); }
INL float sum16(float v) { auto s = __builtin_amdgcn_permlane16_swap(asu(v), asu(v), false, false); return asf(s[0]) + asf(s[1]); }
INL float sum32(float v) { auto s = __builtin_amdgcn_permlane32_swap(asu(v), asu(v), false, false); return asf(s[0]) + asf(s[1]); }
INL float wave_sum(float v) { return sum32(sum16(row_sum(v))); }
INL float red_g(float a, float b) { auto s = __builtin_amdgcn_permlane16_swap(asu(a), asu(b), false, false); return sum32(asf(s[0]) + asf(s[1])); }

#define STAMP(i) do { if (w == 0) { u64 t_; asm volatile("s_memrealtime %0\n\ts_waitcnt lgkmcnt(0)" : "=s"(t_)::"memory"); if (lane == 0) *(LAS u64*)(L + L_STAMP + 8 * (i)) = t_; } } while (0)

struct Sel { int t0, t1; float m0, m1, s0, s1, r0, r1; };

INL void wait_flags(rsrc_t ws, int vo, int n, int lane, u32 tlo, u32 thi) {
  for (u32 sp = 0; sp < SPIN_CAP; ++sp) {
    CLOB();
    v2u f = {tlo, thi};
    if (lane < n) f = __builtin_amdgcn_raw_buffer_load_b64(ws, vo, 0, SC1);
    if (__all(f.x == tlo && f.y == thi)) break;
  }
  asm volatile("" ::: "memory");
}
INL void gather_stats(rsrc_t ws, int so, int lane, u32 tag, float& S, float& Q, float& Dt) {
  for (u32 sp = 0; sp < SPIN_CAP; ++sp) {
    CLOB();
    v4u v[8];
#pragma unroll
    for (int i = 0; i < 8; ++i) v[i] = ld16c(ws, lane * 16, so + i * 1024);
    bool ok = true;
    S = 0.f; Q = 0.f; Dt = 0.f;
#pragma unroll
    for (int i = 0; i < 8; ++i) { ok &= v[i].w == tag; S += asf(v[i].x); Q += asf(v[i].y); Dt += asf(v[i].z); }
    if (__all(ok)) break;
  }
  S = sum32(S); Q = sum32(Q); Dt = sum32(Dt);
}
INL u32 mkkey(float r, int t) {
  u32 b = pk(r, r) & 0xffffu;
  b = (b & 0x8000u) ? (b ^ 0xffffu) : (b | 0x8000u);
  return (b << 16) | (u32)(2047 - t);
}
INL float key_r(u32 k) { u32 b = k >> 16; b = (b & 0x8000u) ? (b & 0x7fffu) : (b ^ 0xffffu); return asf(b << 16); }
INL void publish_cand(rsrc_t ws, int so, int lane, u32 tag, int tb, float r, float mean, float rs) {
  const int c = lane & 15, mt = (lane >> 4) & 1;
  const u32 key = mkkey(r, 32 * tb + 8 * (c >> 2) + 4 * mt + (c & 3));
  const u32 m1 = max16(row_max(key));
  const u32 m2 = max16(row_max(key == m1 ? 0u : key));
  if (lane < 32 && (key == m1 || key == m2)) st16c(v4u{asu(mean), asu(rs), key, tag}, ws, key == m1 ? 0 : 16, so);
}
INL Sel select_top2(rsrc_t ws, int so, int lane, u32 tag) {
  v4u a, b;
  for (u32 sp = 0; sp < SPIN_CAP; ++sp) {
    CLOB();
    a = ld16c(ws, lane * 16, so);
    b = ld16c(ws, lane * 16, so + 1024);
    if (__all(a.w == tag && b.w == tag)) break;
  }
  const u32 hi = umax(a.z, b.z), lo = umin(a.z, b.z);
  const u32 m1 = max32(max16(row_max(hi)));
  const u32 m2 = max32(max16(row_max(hi == m1 ? lo : hi)));
  const int l1 = __builtin_ctzll(__ballot(hi == m1) | (1ull << 63)), l2 = __builtin_ctzll(__ballot(hi == m2 || lo == m2) | (1ull << 63));
  const u32 mA = a.z == m1 ? a.x : b.x, sA = a.z == m1 ? a.y : b.y, mB = a.z == m2 ? a.x : b.x, sB = a.z == m2 ? a.y : b.y;
  Sel s;
  const u32 k1 = (u32)rfl((int)m1), k2 = (u32)rfl((int)m2);
  s.t0 = 2047 - (int)(k1 & 2047u); s.t1 = 2047 - (int)(k2 & 2047u);
  s.r0 = key_r(k1); s.r1 = key_r(k2);
  s.m0 = asf(__builtin_amdgcn_readlane(mA, l1)); s.s0 = asf(__builtin_amdgcn_readlane(sA, l1));
  s.m1 = asf(__builtin_amdgcn_readlane(mB, l2)); s.s1 = asf(__builtin_amdgcn_readlane(sB, l2));
  return s;
}
INL int xf_tok(int t) { const int r = t & 31; return ((t >> 5) * 2 + ((r >> 2) & 1)) * 16384 + (4 * (r >> 3) + (r & 3)) * 16; }
INL u32 tagb(float v, u32 tag, int i) { return (asu(v) & 0xffffff00u) | ((tag >> (8 * i)) & 0xffu); }
INL u32 tagdiff(v4u v, u32 tag) { return ((v.x ^ tag) | (v.y ^ (tag >> 8)) | (v.z ^ (tag >> 16)) | (v.w ^ (tag >> 24))) & 0xffu; }
INL float sigm(float r) { return rbf(1.f / (1.f + __expf(-r))); }

__global__ void __launch_bounds__(256, 1) fnet_mod_kernel(Params P_) {
  u64 te0, te1;
  const __attribute__((address_space(4))) Params* kp = (const __attribute__((address_space(4))) Params*)__builtin_amdgcn_kernarg_segment_ptr();
  asm volatile("s_memrealtime %0\n\ts_waitcnt lgkmcnt(0)" : "=s"(te0), "+s"(kp)::"memory");
  Params P;
  P.x = kp->x; P.Cr = kp->Cr; P.Ci = kp->Ci; P.Dr = kp->Dr; P.Di = kp->Di; P.wr = kp->wr; P.wg = kp->wg; P.wu = kp->wu; P.wd = kp->wd; P.out = kp->out; P.ws = kp->ws;
  asm volatile("s_waitcnt lgkmcnt(0)\n\ts_memrealtime %0\n\ts_waitcnt lgkmcnt(0)" : "=s"(te1) : "s"(P.x), "s"(P.ws) : "memory");
  extern __shared__ __attribute__((aligned(16))) char smem[];
  LAS char* L = (LAS char*)smem;
  int tid = threadIdx.x, lane = tid & 63;
  const int w = rfl(tid >> 6);
  const int b = blockIdx.x, xcd = b & 7, loc = b >> 3;
  const int k2 = xcd & 3, p2 = (xcd >> 2) * 8 + (loc >> 2), j2 = loc & 3, ec = 4 * j2 + k2;
  const int p1 = 4 * (xcd >> 1) + (loc >> 3), i1 = 8 * (xcd & 1) + (loc & 7);
  const int fline = xcd + 8 * (loc >> 3), cg = fline * 8 + (loc & 7), rho = (xcd - 4) * 8 + (loc & 7);
  const bool isGU = fline < 28;
  int c = lane & 15, g = lane >> 4, l16 = lane * 16, tok0 = 0;
  const int tb2 = 4 * p2 + ((w + j2) & 3);
  auto fresh = [&]() { PIN(l16); lane = l16 >> 4; tid = w * 64 + lane; c = lane & 15; g = lane >> 4; tok0 = 32 * tb2 + 8 * (c >> 2) + (c & 3); };
  const rsrc_t ws = mk(P.ws, W_END);
  auto stamp = [&](int i) { STAMP(i); };
  auto cstamp = [&](int i) { if (w == 0) { u64 t_; asm volatile("s_memtime %0\n\ts_waitcnt lgkmcnt(0)" : "=s"(t_)::"memory"); if (lane == 0) *(LAS u64*)(L + L_STAMP + 8 * (i)) = t_; } };
  if (tid == 0) { *(LAS u64*)(L + L_STAMP + 8 * 29) = te0; *(LAS u64*)(L + L_STAMP + 8 * 30) = te1; }
  STAMP(0);

  int vo1[4], vo4[4];
  {
    const int rowl = 8 * (lane >> 5) + ((lane >> 3) & 3), ch0 = ((lane & 7) ^ (lane >> 4)) * 16;
#pragma unroll
    for (int i = 0; i < 4; ++i) {
      const int row = rowl + 16 * (i & 1) + 4 * (i >> 1), ch = (ch0 ^ ((i & 1) * 64)) + 3072 - 1024 * i;
      vo1[i] = row * 1024 + ch; vo4[i] = row * 4096 + ch;
    }
  }
  const rsrc_t rD = mk((const char*)(i1 < 8 ? P.Dr : P.Di) + (i1 & 7) * 65536 - 3072, 65536 + 3072);
  const rsrc_t rX = mk((const char*)P.x + (128 * p1 + 32 * w) * 1024 - 3072, 32768 + 3072);
  const rsrc_t rC = mk((const char*)((k2 >> 1) ? P.Ci : P.Cr) + (size_t)p2 * 128 * 4096 + (k2 & 1) * 2048 + w * 512 - 3072, 127 * 4096 + 512 + 3072);
  const int stg = L_STG + w * 20480;
  auto dmaD = [&](int r) { const int idx = w + 4 * ((r + p1) & 3); dma_unit(L_D + idx * 4096, vo1, rD, (idx >> 3) * 32768 + (idx & 7) * 128); };
  auto dmaX = [&](int u) { dma_unit(stg + (u % 5) * 4096, vo1, rX, ((u + i1) & 7) * 128); };
  auto dmaC = [&](int n) { dma_unit(stg + ((8 + n) % 5) * 4096, vo4, rC, ((w + j2 + 1 + (n >> 2)) & 3) * 131072 + (n & 3) * 128); };
  asm volatile("s_nop 4" ::: "memory");
  dmaD(0); dmaX(0);
  const v2u ep = __builtin_amdgcn_raw_buffer_load_b64(ws, 0, W_EPOCH, SC1);
  dmaD(1); dmaD(2); dmaD(3);
  dmaX(1); dmaX(2); dmaX(3); dmaX(4);
  int A0 = c * 128 + ((g ^ (c >> 1)) * 16), A1 = A0 ^ 64;
  v4u ones = {0x3f803f80u, 0x3f803f80u, 0x3f803f80u, 0x3f803f80u};
  PIN(ones);
  VMW(16);
  asm volatile("s_barrier" ::: "memory");
  STAMP(1);
  {
    v4f r;
    const int base = L_D + (w >> 1) * 32768 + (w & 1) * 2048;
    sfor<0, 16>([&](auto I) {
      constexpr int i = I.v;
      v4u d = *(const LAS v4u*)(L + base + ((i & 1) ? A1 : A0) + (i >> 1) * 4096);
      if constexpr (i == 0) MFMA0(r, ones, d); else MFMA(r, ones, d);
    });
    asm volatile("s_nop 7" : "+v"(r));
    if (g == 0) *(LAS float*)(L + L_RS + (w * 16 + c) * 4) = r[0];
  }
  v4f acc[8], accS[2], accQ[2];
  sfor<0, 8>([&](auto U) {
    constexpr int u = U.v;
    VMW(16);
    v4u xf[4], df[8];
    const int sl = stg + (u % 5) * 4096, dk = L_D + ((u + i1) & 7) * 4096;
#pragma unroll
    for (int i = 0; i < 4; ++i) xf[i] = *(const LAS v4u*)(L + sl + ((i & 1) ? A1 : A0) + (i >> 1) * 2048);
#pragma unroll
    for (int i = 0; i < 4; ++i) PIN(xf[i]);
    LGK0();
    if constexpr (u + 5 < 8) dmaX(u + 5); else dmaC(u + 5 - 8);
#pragma unroll
    for (int i = 0; i < 8; ++i) df[i] = *(const LAS v4u*)(L + dk + ((i >> 2) ? A1 : A0) + ((i >> 1) & 1) * 32768 + (i & 1) * 2048);
    sfor<0, 2>([&](auto KB_) {
      constexpr int kb = KB_.v;
      sfor<0, 2>([&](auto MT) {
        constexpr int mt = MT.v;
        if constexpr (u == 0 && kb == 0) { MFMA0(accS[mt], xf[mt * 2 + kb], ones); MFMA0(accQ[mt], xf[mt * 2 + kb], xf[mt * 2 + kb]); }
        else { MFMA(accS[mt], xf[mt * 2 + kb], ones); MFMA(accQ[mt], xf[mt * 2 + kb], xf[mt * 2 + kb]); }
      });
      sfor<0, 8>([&](auto I) {
        constexpr int i = I.v, mt = i & 1, nn = i >> 1;
        if constexpr (u == 0 && kb == 0) MFMA0(acc[mt * 4 + nn], xf[mt * 2 + kb], df[kb * 4 + nn]);
        else MFMA(acc[mt * 4 + nn], xf[mt * 2 + kb], df[kb * 4 + nn]);
      });
    });
  });
  asm volatile("s_nop 7" : "+v"(acc[0]), "+v"(acc[1]), "+v"(acc[2]), "+v"(acc[3]), "+v"(acc[4]), "+v"(acc[5]), "+v"(acc[6]), "+v"(acc[7]),
               "+v"(accS[0]), "+v"(accS[1]), "+v"(accQ[0]), "+v"(accQ[1]));
  BAR();
  STAMP(2);
  float RS[4];
#pragma unroll
  for (int i = 0; i < 4; ++i) RS[i] = *(const LAS float*)(L + L_RS + (i * 16 + c) * 4);
  const float sgn = i1 < 8 ? 1.f : -1.f;
  auto gemm1_out = [&](const float (&mean)[8], const float (&sc)[8], int base) {
#pragma unroll
    for (int nn = 0; nn < 4; ++nn) {
      float v[8];
#pragma unroll
      for (int q = 0; q < 8; ++q) v[q] = (acc[(q >> 2) * 4 + nn][q & 3] - mean[q] * RS[nn]) * sc[q];
      st16c(v4u{pk(v[0], v[1]), pk(v[2], v[3]), pk(v[4], v[5]), pk(v[6], v[7])}, ws, l16, base + ((4 * i1 + nn) * 64 + 4 * p1 + w) * 1024);
    }
  };
  {
    float mean[8], sc[8];
#pragma unroll
    for (int q = 0; q < 8; ++q) {
      const float Q = asf(__builtin_amdgcn_ds_bpermute((20 * g + (q & 3)) * 4, asu(accQ[q >> 2][q & 3])));
      mean[q] = accS[q >> 2][q & 3] * (1.f / 512);
      sc[q] = sgn * __builtin_amdgcn_rsqf(fmaxf(Q * (1.f / 512) - mean[q] * mean[q], 0.f) + EPS);
    }
    gemm1_out(mean, sc, W_ABF1);
  }
  const u64 epoch = (((u64)(u32)rfl((int)ep.y) << 32) | (u32)rfl((int)ep.x)) + 1;
  const u32 tag = (u32)epoch, tagh = (u32)(epoch >> 32);
  {
    const v4u z = {tag - 1, tag - 1, tag - 1, tag - 1};
    if (w == 0 && lane < 40) {
      const int i = lane >> 3;
      const int off = i == 0 ? W_F2 + (p2 * 16 + ec) * 128 : i == 1 ? W_F3 + (p1 * 16 + i1) * 128 : i == 2 ? W_CAND2 + (i1 * 16 + p1) * 128
                    : i == 3 ? W_CAND4 + (ec * 16 + p2) * 128 : W_DAB + b * 128;
      st16c(z, ws, off + (lane & 7) * 16, 0);
    }
  }
  STAMP(3);
  v4u cf[64];
  DRAIN();
  asm volatile("s_barrier" ::: "memory");
  if (tid < 8) st16c(v4u{tag, tagh, tag, tagh}, ws, l16, W_F1 + (p1 * 16 + i1) * 128);
  STAMP(4);
  sfor<0, 16>([&](auto N) {
    constexpr int n = N.v;
    if constexpr (n >= 5) VMW(n + 4 < 16 ? 16 : (15 - n) * 4);
    const int sl = stg + ((8 + n) % 5) * 4096;
    sfor<0, 4>([&](auto I) {
      constexpr int i = I.v;
      cf[(n >> 2) * 16 + (i >> 1) * 8 + (n & 3) * 2 + (i & 1)] = *(const LAS v4u*)(L + sl + ((i & 1) ? A1 : A0) + (i >> 1) * 2048);
    });
    sfor<0, 4>([&](auto I) { constexpr int i = I.v; PINA(cf[(n >> 2) * 16 + (i >> 1) * 8 + (n & 3) * 2 + (i & 1)]); });
    LGK0();
    if constexpr (n + 5 < 16) dmaC(n + 5);
  });
  STAMP(5);

  auto fvo = [&]() { return ((8 * (k2 & 1) + 2 * w + ((lane >> 1) & 1)) * 16 + 8 * (k2 >> 1) + 2 * j2 + (lane & 1)) * 128; };

  auto gemm2 = [&](int abf, int exc, float (&y)[16], auto&& hook) {
    int redW[3];
#pragma unroll
    for (int u = 0; u < 3; ++u) redW[u] = L_RED + (((w + 1 + u) & 3) * 3 + u) * 4096 + l16;
    const int redR = L_RED + w * 12288 + l16;
    v4u ab[2][16];
    v4f a[2][4];
    float base[16], bx[16];
    v4u xe[9];
    auto abso = [&](int i, int f) { return abf + (((16 * (k2 >> 1) + 4 * j2 + ((k2 + 1 + i) & 3)) * 2 + (f >> 3)) * 64 + 32 * (k2 & 1) + 8 * w + (f & 7)) * 1024; };
    const int excin = exc + ((p2 * 16 + ec) * 12 + w) * 3072;
#pragma unroll
    for (int f = 0; f < 16; ++f) ab[0][f] = ld16c(ws, l16, abso(0, f));
    sfor<0, 4>([&](auto I) {
      constexpr int i = I.v;
      v4f rd[12];
      u32 pkd[8];
      sfor<0, 128>([&](auto N) {
        constexpr int n = N.v, u = n >> 5, ch = (n >> 3) & 3, ks = n & 7;
        if constexpr (ks == 0) MFMA0A(a[u & 1][ch], ab[i & 1][(ch & 1) * 8 + ks], cf[u * 16 + (ch >> 1) * 8 + ks]);
        else MFMAA(a[u & 1][ch], ab[i & 1][(ch & 1) * 8 + ks], cf[u * 16 + (ch >> 1) * 8 + ks]);
        if constexpr (i > 0 && n >= 8 && n < 24) VADD(base[n - 8], a[1][(n - 8) >> 2][(n - 8) & 3]);
        if constexpr (i > 0 && n >= 18 && n < 26) { pkd[n - 18] = pk(base[2 * (n - 18)], base[2 * (n - 18) + 1]); PIN(pkd[n - 18]); }
        if constexpr (i > 0 && n >= 26 && n < 29) {
          constexpr int q = n - 26;
          st16c(v4u{pkd[3 * q], pkd[3 * q + 1], q < 2 ? pkd[(3 * q + 2) & 7] : tag, tag}, ws, l16,
                exc + (((p2 * 16 + 4 * j2 + ((k2 + i) & 3)) * 3 + (i - 1)) * 4 + w) * 3072 + q * 1024);
        }
        if constexpr (n == 14) BAR();
        if constexpr (n >= 15 && n <= 103 && (n & 7) == 7) {
          constexpr int m = (n - 15) >> 3;
          PIN(a[(m >> 2) & 1][m & 3]);
          *(LAS v4f*)(L + redW[m >> 2] + (m & 3) * 1024) = a[(m >> 2) & 1][m & 3];
        }
        if constexpr (n == 105) BAR();
        if constexpr (n >= 105 && n < 117) rd[n - 105] = *(const LAS v4f*)(L + redR + (((n - 105) % 3) * 4 + (n - 105) / 3) * 1024);
        if constexpr (n >= 112) {
          constexpr int q = n - 112;
          VADD3(base[q], rd[(q >> 2) * 3][q & 3], rd[(q >> 2) * 3 + 1][q & 3]);
          VADD(base[q], rd[(q >> 2) * 3 + 2][q & 3]);
        }
        if constexpr (i < 3 && n >= 30 && n < 94 && (n & 3) == 2) ab[(i + 1) & 1][(n - 30) >> 2] = ld16c(ws, l16, abso(i + 1, (n - 30) >> 2));
        if constexpr (i == 3 && n >= 30 && n < 36) xe[n - 30] = ld16c(ws, l16, excin + ((n - 30) / 3) * 12288 + ((n - 30) % 3) * 1024);
        if constexpr (i == 3 && n >= 72 && n < 75) xe[n - 66] = ld16c(ws, l16, excin + 2 * 12288 + (n - 72) * 1024);
        hook(I, N);
      });
    });
    bool ok = true;
#pragma unroll
    for (int q = 0; q < 9; ++q) ok &= xe[q].w == tag;
    for (u32 sp = 0; !__all(ok) && sp < SPIN_CAP; ++sp) {
      CLOB();
      ok = true;
#pragma unroll
      for (int q = 0; q < 9; ++q) xe[q] = ld16c(ws, l16, excin + (q / 3) * 12288 + (q % 3) * 1024);
#pragma unroll
      for (int q = 0; q < 9; ++q) ok &= xe[q].w == tag;
    }
#pragma unroll
    for (int q = 0; q < 8; ++q) {
      const u32 d0 = xe[q / 3][q % 3], d1 = xe[3 + q / 3][q % 3], d2 = xe[6 + q / 3][q % 3];
      bx[2 * q] = lo16(d0) + lo16(d1) + lo16(d2);
      bx[2 * q + 1] = hi16(d0) + hi16(d1) + hi16(d2);
    }
    asm volatile("s_nop 7" : "+v"(a[1][0]), "+v"(a[1][1]), "+v"(a[1][2]), "+v"(a[1][3]));
#pragma unroll
    for (int q = 0; q < 16; ++q) y[q] = (a[1][q >> 2][q & 3] + base[q]) + bx[q];
  };
  const rsrc_t rW = mk(P.wr, 1024);
  auto tile_out = [&](const float (&v)[16], u32 (&xp)[8], int xf, int stat, bool drain_first) {
    const v4u wq = ld16(rW, g * 16, ec * 64);
#pragma unroll
    for (int q = 0; q < 8; ++q) xp[q] = pk(v[2 * q], v[2 * q + 1]);
    st16c(v4u{xp[0], xp[1], xp[2], xp[3]}, ws, l16, xf + ((tb2 * 2 + 0) * 16 + ec) * 1024);
    st16c(v4u{xp[4], xp[5], xp[6], xp[7]}, ws, l16, xf + ((tb2 * 2 + 1) * 16 + ec) * 1024);
    float s[2], q2[2], d[2];
#pragma unroll
    for (int mt = 0; mt < 2; ++mt) {
      s[mt] = 0.f; q2[mt] = 0.f; d[mt] = 0.f;
#pragma unroll
      for (int q = 0; q < 4; ++q) {
        const float a0 = lo16(xp[mt * 4 + q]), a1 = hi16(xp[mt * 4 + q]);
        s[mt] += a0 + a1; q2[mt] += a0 * a0 + a1 * a1; d[mt] += a0 * lo16(wq[q]) + a1 * hi16(wq[q]);
      }
    }
    const float S = red_g(s[0], s[1]), Q = red_g(q2[0], q2[1]), Dt = red_g(d[0], d[1]);
    if (drain_first) DRAIN();
    if (lane < 32) st16c(v4u{asu(S), asu(Q), asu(Dt), tag}, ws, l16, stat + (tb2 * 16 + ec) * 512);
  };

  fresh();
  wait_flags(ws, W_F1 + fvo(), 4, lane, tag, tagh);
  STAMP(6);
  u32 x1p[8];
  {
    float y[16];
    v4u xr[2];
    const rsrc_t rXa = mk(P.x, 2 * MB);
    const rsrc_t rG = mk((const char*)((w >> 1) ? P.wu : P.wg) + cg * 16, isGU ? 512 * 3584 - cg * 16 : 0);
    const rsrc_t rWd = mk((const char*)P.wd + cg * 8192, isGU ? 8192 : 0);
    const int pos = lane & 31;
    const int gvo = (32 * (lane >> 5) + 8 * ((pos >> 2) & 3) + 4 * (pos >> 4) + (pos & 3)) * 3584;
    gemm2(W_ABF1, W_EXC1, y, [&](auto I, auto N) {
      constexpr int i = I.v, n = N.v;
      if constexpr (i == 3 && (n == 40 || n == 41)) xr[n - 40] = ld16(rXa, (tok0 + 4 * (n - 40)) * 1024 + g * 16, ec * 64);
      if constexpr ((i == 1 || i == 2) && (n == 96 || n == 100)) {
        constexpr int q = (i - 1) * 2 + (n - 96) / 4;
        dma_one(((w >> 1) ? L_WU : L_WG) + ((w & 1) * 4 + q) * 1024, gvo, rG, ((w & 1) * 4 + q) * 64 * 3584);
      }
      if constexpr ((i == 1 || i == 2) && n == 98) dma_one(L_WD + (w * 2 + i - 1) * 1024, l16, rWd, (w * 2 + i - 1) * 1024);
    });
    STAMP(7);
    fresh();
#pragma unroll
    for (int q = 0; q < 8; ++q) { y[2 * q] += lo16(xr[q >> 2][q & 3]); y[2 * q + 1] += hi16(xr[q >> 2][q & 3]); }
    tile_out(y, x1p, W_XF1, W_STAT1, false);
  }
  DRAIN();
  asm volatile("s_barrier" ::: "memory");
  if (tid < 8) st16c(v4u{tag, tagh, tag, tagh}, ws, l16, W_F2 + (p2 * 16 + ec) * 128);
  STAMP(8);
  float Wsum;
  {
    const v4u wq = ld16(rW, l16, 0);
    float s = 0.f;
#pragma unroll
    for (int q = 0; q < 4; ++q) s += lo16(wq[q]) + hi16(wq[q]);
    Wsum = rflf(wave_sum(s));
  }
  auto wfrag = [&](int kb) {
    const int trA = ((c >> 1) & 1 ? L_WU : L_WG) + (4 * g + (c >> 2)) * 16 + (c & 1) * 8;
    const v4s lo = __builtin_amdgcn_ds_read_tr16_b64_v4i16((LAS v4s*)(L + trA + kb * 512));
    const v4s hi = __builtin_amdgcn_ds_read_tr16_b64_v4i16((LAS v4s*)(L + trA + kb * 512 + 256));
    const v2u a = __builtin_bit_cast(v2u, lo), bb = __builtin_bit_cast(v2u, hi);
    return v4u{a.x, a.y, bb.x, bb.y};
  };
  auto expert_front = [&](int cand, int xf, int py) {
    if (w == 0) {
      const Sel s = select_top2(ws, cand + (b & 15) * 2048, lane, tag);
      if (isGU) {
        v4u xr[16];
        const int vo = ((c & 1) ? xf_tok(s.t1) : xf_tok(s.t0)) + g * 256;
#pragma unroll
        for (int kb = 0; kb < 16; ++kb) xr[kb] = ld16c(ws, vo, xf + kb * 1024);
        v4f h, cs;
        ones = v4u{0x3f803f80u, 0x3f803f80u, 0x3f803f80u, 0x3f803f80u};
        PIN(ones);
        sfor<0, 16>([&](auto K) {
          constexpr int kb = K.v;
          v4u wf = wfrag(kb);
          asm volatile("s_nop 1" : "+v"(wf));
          if constexpr (kb == 0) { MFMA0(cs, wf, ones); MFMA0(h, wf, xr[kb]); } else { MFMA(cs, wf, ones); MFMA(h, wf, xr[kb]); }
        });
        asm volatile("s_nop 7" : "+v"(h), "+v"(cs));
        const float mean = (c & 1) ? s.m1 : s.m0, rs = (c & 1) ? s.s1 : s.s0;
        v4f o;
#pragma unroll
        for (int i = 0; i < 4; ++i) {
          const float v = rbf((h[i] - mean * cs[i]) * rs);
          const auto sw = __builtin_amdgcn_permlane32_swap(asu(v), asu(v), false, false);
          const float gt = asf(sw[0]), up = asf(sw[1]);
          o[i] = rbf(rbf(gt / (1.f + __expf(-gt))) * up);
        }
        if (c < 2 && g < 2) *(LAS v4f*)(L + L_H + c * 32 + g * 16) = o;
      }
      if (lane == 0) {
        *(LAS v4u*)(L + L_H + 64) = v4u{(u32)s.t0, (u32)s.t1, asu(s.m0), asu(s.m1)};
        *(LAS v4u*)(L + L_H + 80) = v4u{asu(s.s0), asu(s.s1), asu(s.r0), asu(s.r1)};
      }
    }
    u32 wd[8];
#pragma unroll
    for (int i = 0; i < 8; ++i) wd[i] = *(const LAS u32*)(L + L_WD + i * 1024 + tid * 4);
    BAR();
    if (isGU) {
      float y[4] = {0.f, 0.f, 0.f, 0.f};
#pragma unroll
      for (int q = 0; q < 4; ++q) {
        const v4f h = *(const LAS v4f*)(L + L_H + q * 16);
#pragma unroll
        for (int i = 0; i < 4; ++i) { y[(q >> 1) * 2] += h[i] * lo16(wd[(q & 1) * 4 + i]); y[(q >> 1) * 2 + 1] += h[i] * hi16(wd[(q & 1) * 4 + i]); }
      }
      st16c(v4u{tagb(y[0], tag, 0), tagb(y[1], tag, 1), tagb(y[2], tag, 2), tagb(y[3], tag, 3)}, ws, tid * 16, py + cg * 4096);
    }
    const v4u s0 = *(const LAS v4u*)(L + L_H + 64), s1 = *(const LAS v4u*)(L + L_H + 80);
    Sel s;
    s.t0 = rfl((int)s0.x); s.t1 = rfl((int)s0.y); s.m0 = rflf(asf(s0.z)); s.m1 = rflf(asf(s0.w));
    s.s0 = rflf(asf(s1.x)); s.s1 = rflf(asf(s1.y)); s.r0 = rflf(asf(s1.z)); s.r1 = rflf(asf(s1.w));
    return s;
  };
  auto expert_back = [&](int py, const Sel& s, float (&u)[4]) {
    v4f t;
    for (u32 sp = 0; sp < SPIN_CAP; ++sp) {
    CLOB();
      v4u v[7];
#pragma unroll
      for (int i = 0; i < 7; ++i) v[i] = ld16c(ws, (tid >> 3) * 4096 + (tid & 7) * 16, py + rho * 128 + i * 131072);
      u32 bad = 0;
      t = v4f{0.f, 0.f, 0.f, 0.f};
#pragma unroll
      for (int i = 0; i < 7; ++i) { bad |= tagdiff(v[i], tag); t += __builtin_bit_cast(v4f, v[i] & 0xffffff00u); }
      if (__all(bad == 0)) break;
    }
#pragma unroll
    for (int i = 0; i < 4; ++i) t[i] = sum32(sum16(t[i] + dppf<0x128>(t[i])));
    if (lane < 8) *(LAS v4f*)(L + L_X + w * 128 + l16) = t;
    BAR();
    t = v4f{0.f, 0.f, 0.f, 0.f};
#pragma unroll
    for (int i = 0; i < 4; ++i) t += *(const LAS v4f*)(L + L_X + i * 128 + (lane & 7) * 16);
    const float g0 = sigm(s.r0), g1 = sigm(s.r1);
    u[0] = rbf(rbf(t[0]) * g0); u[1] = rbf(rbf(t[1]) * g0); u[2] = rbf(rbf(t[2]) * g1); u[3] = rbf(rbf(t[3]) * g1);
  };

  DRAIN();
  if (w == 0) wait_flags(ws, W_F2 + (p1 * 16 + c) * 128, 16, lane, tag, tagh);
  asm volatile("s_barrier" ::: "memory");
  STAMP(9);
  {
    const int tb1 = 4 * p1 + w;
    A0 = c * 128 + ((g ^ (c >> 1)) * 16); A1 = A0 ^ 64;
#pragma unroll
    for (int i = 0; i < 4; ++i) RS[i] = *(const LAS float*)(L + L_RS + (i * 16 + c) * 4);
    v4u sv[8], xf[32];
#pragma unroll
    for (int i = 0; i < 8; ++i) sv[i] = ld16c(ws, l16, W_STAT1 + tb1 * 8192 + i * 1024);
#pragma unroll
    for (int i = 0; i < 32; ++i) xf[i] = ld16c(ws, l16, W_XF1 + ((tb1 * 2 + ((i >> 1) & 1)) * 16 + 2 * (((i >> 2) + i1) & 7) + (i & 1)) * 1024);
    __builtin_amdgcn_sched_barrier(0);
    float S = 0.f, Q = 0.f, Dt = 0.f;
#pragma unroll
    for (int i = 0; i < 8; ++i) { S += asf(sv[i].x); Q += asf(sv[i].y); Dt += asf(sv[i].z); }
    S = sum32(S); Q = sum32(Q); Dt = sum32(Dt);
    const float mean = S * (1.f / 512), var = fmaxf(Q * (1.f / 512) - mean * mean, 0.f), rs = __builtin_amdgcn_rsqf(var + EPS);
    publish_cand(ws, W_CAND2 + (i1 * 16 + p1) * 128 + w * 32, lane, tag, tb1, (Dt - mean * Wsum) * rs, mean, rs);
    const float c3 = (1.f + rs) * __builtin_amdgcn_rsqf(var * (1.f + rs) * (1.f + rs) + EPS);
    if (lane < 32) { *(LAS float*)(L + L_TR + w * 1024 + lane * 4) = mean; *(LAS float*)(L + L_TR + w * 1024 + 128 + lane * 4) = c3 * sgn; }
    sfor<0, 8>([&](auto U) {
      constexpr int u = U.v;
      v4u df[8];
      const int dk = L_D + ((u + i1) & 7) * 4096;
#pragma unroll
      for (int i = 0; i < 8; ++i) df[i] = *(const LAS v4u*)(L + dk + ((i >> 2) ? A1 : A0) + ((i >> 1) & 1) * 32768 + (i & 1) * 2048);
      sfor<0, 16>([&](auto I) {
        constexpr int i = I.v, kb = i >> 3, mt = i & 1, nn = (i >> 1) & 3;
        if constexpr (u == 0 && kb == 0) MFMA0(acc[mt * 4 + nn], xf[u * 4 + mt * 2 + kb], df[kb * 4 + nn]);
        else MFMA(acc[mt * 4 + nn], xf[u * 4 + mt * 2 + kb], df[kb * 4 + nn]);
      });
    });
    float mn[8], sc[8];
#pragma unroll
    for (int mt = 0; mt < 2; ++mt) {
      const v4f a = *(const LAS v4f*)(L + L_TR + w * 1024 + mt * 64 + g * 16), bb = *(const LAS v4f*)(L + L_TR + w * 1024 + 128 + mt * 64 + g * 16);
#pragma unroll
      for (int i = 0; i < 4; ++i) { mn[mt * 4 + i] = a[i]; sc[mt * 4 + i] = bb[i]; }
    }
    asm volatile("s_nop 7" : "+v"(acc[0]), "+v"(acc[1]), "+v"(acc[2]), "+v"(acc[3]), "+v"(acc[4]), "+v"(acc[5]), "+v"(acc[6]), "+v"(acc[7]));
    STAMP(10);
    gemm1_out(mn, sc, W_ABF2);
  }
  DRAIN();
  asm volatile("s_barrier" ::: "memory");
  if (tid < 8) st16c(v4u{tag, tagh, tag, tagh}, ws, l16, W_F3 + (p1 * 16 + i1) * 128);
  STAMP(11);

  const Sel s2 = expert_front(W_CAND2, W_XF1, W_PY2);
  STAMP(12);
  if (!isGU) {
    float u[4];
    expert_back(W_PY2, s2, u);
    if (tid < 8) st16c(v4u{tagb(u[0], tag, 0), tagb(u[1], tag, 1), tagb(u[2], tag, 2), tagb(u[3], tag, 3)}, ws, l16, W_UPD + rho * 128);
  }
  {
    const int n = 4 * b + w;
    const rsrc_t rDn = mk((const char*)(n < 512 ? P.Dr : P.Di) + (n & 511) * 1024, 1024);
    u32 dn[4], xa[4], xb[4];
    const int xo = (lane >> 4) * 1024 + ((lane >> 2) & 3) * 256 + (lane & 3) * 4;
#pragma unroll
    for (int i = 0; i < 4; ++i) {
      dn[i] = __builtin_amdgcn_raw_buffer_load_b32(rDn, lane * 4, i * 256, 0);
      xa[i] = __builtin_amdgcn_raw_buffer_load_b32(ws, xo, W_XF1 + xf_tok(s2.t0) + i * 4096, SC1);
      xb[i] = __builtin_amdgcn_raw_buffer_load_b32(ws, xo, W_XF1 + xf_tok(s2.t1) + i * 4096, SC1);
    }
    v4u up[4];
    for (u32 sp = 0; sp < SPIN_CAP; ++sp) {
    CLOB();
      u32 bad = 0;
#pragma unroll
      for (int i = 0; i < 4; ++i) up[i] = ld16c(ws, l16, W_UPD + i * 1024);
#pragma unroll
      for (int i = 0; i < 4; ++i) bad |= tagdiff(up[i], tag);
      if (__all(bad == 0)) break;
    }
    float dl[2], sD = 0.f;
#pragma unroll
    for (int i = 0; i < 4; ++i) sD += lo16(dn[i]) + hi16(dn[i]);
    sD = wave_sum(sD);
#pragma unroll
    for (int t = 0; t < 2; ++t) {
      const float m = t ? s2.m1 : s2.m0, rs = t ? s2.s1 : s2.s0;
      float s = 0.f, q = 0.f, dnew = 0.f, dold = 0.f;
#pragma unroll
      for (int i = 0; i < 4; ++i) {
        const u32 xx = t ? xb[i] : xa[i];
#pragma unroll
        for (int h = 0; h < 2; ++h) {
          const float x1 = h ? hi16(xx) : lo16(xx), dd = h ? hi16(dn[i]) : lo16(dn[i]);
          const float x2 = rbf(x1 + rbf(rbf((x1 - m) * rs) + hi16(up[i][2 * t + h])));
          s += x2; q += x2 * x2; dnew += x2 * dd; dold += x1 * dd;
        }
      }
      s = wave_sum(s); q = wave_sum(q); dnew = wave_sum(dnew); dold = wave_sum(dold);
      const float m2 = s * (1.f / 512), rs2 = __builtin_amdgcn_rsqf(fmaxf(q * (1.f / 512) - m2 * m2, 0.f) + EPS);
      const float var = fmaxf(1.f / (rs * rs) - EPS, 0.f), c3 = (1.f + rs) * __builtin_amdgcn_rsqf(var * (1.f + rs) * (1.f + rs) + EPS);
      dl[t] = ((dnew - m2 * sD) * rs2 - (dold - m * sD) * c3) * (n < 512 ? 1.f : -1.f);
    }
    if (lane == 0) st16c(v4u{asu(dl[0]), asu(dl[1]), tag, tag}, ws, 0, W_DAB + b * 128 + w * 16);
  }
  STAMP(13);
  float res[16];
  {
    float S, Q, Dt;
    gather_stats(ws, W_STAT1 + tb2 * 8192, lane, tag, S, Q, Dt);
    const float mean = S * (1.f / 512), rs = __builtin_amdgcn_rsqf(fmaxf(Q * (1.f / 512) - mean * mean, 0.f) + EPS);
    const auto sm = __builtin_amdgcn_permlane16_swap(asu(mean), asu(mean), false, false), sr = __builtin_amdgcn_permlane16_swap(asu(rs), asu(rs), false, false);
    const rsrc_t rCr = mk(P.Cr, 8 * MB), rCi = mk(P.Ci, 8 * MB);
    float cc[2][4];
#pragma unroll
    for (int mt = 0; mt < 2; ++mt) {
      const int ro = (tok0 + 4 * mt) * 4096;
      cc[mt][0] = lo16(__builtin_amdgcn_raw_buffer_load_b16(rCr, ro, s2.t0 * 2, 0));
      cc[mt][1] = lo16(__builtin_amdgcn_raw_buffer_load_b16(rCr, ro, s2.t1 * 2, 0));
      cc[mt][2] = lo16(__builtin_amdgcn_raw_buffer_load_b16(rCi, ro, s2.t0 * 2, 0));
      cc[mt][3] = lo16(__builtin_amdgcn_raw_buffer_load_b16(rCi, ro, s2.t1 * 2, 0));
    }
    v4u up[4];
#pragma unroll
    for (int i = 0; i < 4; ++i) up[i] = ld16c(ws, g * 64 + i * 16, W_UPD + ec * 256);
    v4u dv;
    for (u32 sp = 0; sp < SPIN_CAP; ++sp) {
    CLOB();
      dv = ld16c(ws, (lane >> 5) * 16384 + ((lane >> 2) & 7) * 128 + (lane & 3) * 16, W_DAB + ec * 1024);
      if (__all(dv.z == tag)) break;
    }
    *(LAS v2u*)(L + L_TR + w * 1024 + lane * 8) = v2u{dv.x, dv.y};
    LGK0();
#pragma unroll
    for (int q = 0; q < 4; ++q) {
      const v4f da = *(const LAS v4f*)(L + L_TR + w * 1024 + g * 64 + q * 16), db = *(const LAS v4f*)(L + L_TR + w * 1024 + 256 + g * 64 + q * 16);
#pragma unroll
      for (int mt = 0; mt < 2; ++mt) {
        const float m = asf(sm[mt]), r = asf(sr[mt]);
        const int t = tok0 + 4 * mt;
#pragma unroll
        for (int h = 0; h < 2; ++h) {
          const float x1 = h ? hi16(x1p[mt * 4 + q]) : lo16(x1p[mt * 4 + q]);
          const float u = t == s2.t0 ? hi16(up[q][h]) : t == s2.t1 ? hi16(up[q][2 + h]) : 0.f;
          const float x2 = rbf(x1 + rbf(rbf((x1 - m) * r) + u));
          res[mt * 8 + q * 2 + h] = x2 + (cc[mt][0] * da[2 * h] + cc[mt][1] * da[2 * h + 1] + cc[mt][2] * db[2 * h] + cc[mt][3] * db[2 * h + 1]);
        }
      }
    }
  }
#pragma unroll
  for (int q = 0; q < 16; ++q) PIN(res[q]);
  fresh();
  STAMP(14);
  wait_flags(ws, W_F3 + fvo(), 4, lane, tag, tagh);
  STAMP(15);
  u32 x3p[8];
  {
    float y[16];
    gemm2(W_ABF2, W_EXC2, y, [&](auto I, auto N) {
      if constexpr (N.v == 15) stamp(21 + 2 * I.v);
      if constexpr (N.v == 104) stamp(22 + 2 * I.v);
      if constexpr (N.v == 15 && I.v == 0) ;
      if constexpr (N.v == 104 && I.v == 3) ;
    });
    STAMP(16);
    fresh();
#pragma unroll
    for (int q = 0; q < 16; ++q) y[q] += res[q];
    tile_out(y, x3p, W_XF3, W_STAT3, true);
  }
  DRAIN();
  STAMP(17);
  v4u o[2];
  {
    float S, Q, Dt;
    gather_stats(ws, W_STAT3 + tb2 * 8192, lane, tag, S, Q, Dt);
    const float mean = S * (1.f / 512), rs = __builtin_amdgcn_rsqf(fmaxf(Q * (1.f / 512) - mean * mean, 0.f) + EPS);
    publish_cand(ws, W_CAND4 + (ec * 16 + p2) * 128 + (tb2 & 3) * 32, lane, tag, tb2, (Dt - mean * Wsum) * rs, mean, rs);
    const auto sm = __builtin_amdgcn_permlane16_swap(asu(mean), asu(mean), false, false), sr = __builtin_amdgcn_permlane16_swap(asu(rs), asu(rs), false, false);
#pragma unroll
    for (int q = 0; q < 8; ++q) {
      const float m = asf(sm[q >> 2]), r = asf(sr[q >> 2]), a0 = lo16(x3p[q]), a1 = hi16(x3p[q]);
      o[q >> 2][q & 3] = pk(a0 + rbf((a0 - m) * r), a1 + rbf((a1 - m) * r));
    }
  }
  STAMP(18);
  DRAIN();
  const Sel s4 = expert_front(W_CAND4, W_XF3, W_PY4);
  STAMP(19);
  const rsrc_t rO = mk(P.out, 2 * MB);
#pragma unroll
  for (int mt = 0; mt < 2; ++mt) {
    const int t = tok0 + 4 * mt;
    if (t != s4.t0 && t != s4.t1) st16(o[mt], rO, t * 1024 + g * 16, ec * 64);
  }
  if (b == 0 && tid == 0) __builtin_amdgcn_raw_buffer_store_b64(v2u{tag, tagh}, ws, 0, W_EPOCH, SC1);
  if (!isGU) {
    const int d = rho * 16 + (lane & 7) * 2, xo = (d >> 5) * 1024 + ((d >> 3) & 3) * 256 + (d & 7) * 2;
    const u32 xa = __builtin_amdgcn_raw_buffer_load_b32(ws, xo, W_XF3 + xf_tok(s4.t0), SC1);
    const u32 xb = __builtin_amdgcn_raw_buffer_load_b32(ws, xo, W_XF3 + xf_tok(s4.t1), SC1);
    float u[4];
    expert_back(W_PY4, s4, u);
    if (tid < 8) {
      const u32 oa = pk(lo16(xa) + rbf(rbf((lo16(xa) - s4.m0) * s4.s0) + u[0]), hi16(xa) + rbf(rbf((hi16(xa) - s4.m0) * s4.s0) + u[1]));
      const u32 ob = pk(lo16(xb) + rbf(rbf((lo16(xb) - s4.m1) * s4.s1) + u[2]), hi16(xb) + rbf(rbf((hi16(xb) - s4.m1) * s4.s1) + u[3]));
      __builtin_amdgcn_raw_buffer_store_b32(oa, rO, d * 2, s4.t0 * 1024, 0);
      __builtin_amdgcn_raw_buffer_store_b32(ob, rO, d * 2, s4.t1 * 1024, 0);
    }
  }
  DRAIN();
  STAMP(20);
  if (w == 0 && b == 100) {
    const u64 ta = *(LAS u64*)(L + L_STAMP + 8 * 29), tb = *(LAS u64*)(L + L_STAMP + 8 * AMP);
    const u64 end = __builtin_amdgcn_s_memrealtime() + (tb - ta) * 20;
    while (__builtin_amdgcn_s_memrealtime() < end) __builtin_amdgcn_s_sleep(2);
  }
}

extern "C" void kernel_launch(void* const* d_in, const int* in_sizes, int n_in, void* d_out, int out_size, void* d_ws, size_t ws_size,
                              hipStream_t stream) {
  Params p;
  p.x = (const u16*)d_in[0]; p.Cr = (const u16*)d_in[1]; p.Ci = (const u16*)d_in[2]; p.Dr = (const u16*)d_in[3]; p.Di = (const u16*)d_in[4];
  p.wr = (const u16*)d_in[6]; p.wg = (const u16*)d_in[7]; p.wu = (const u16*)d_in[8]; p.wd = (const u16*)d_in[9];
  p.out = (u16*)d_out; p.ws = (char*)d_ws;
  (void)hipFuncSetAttribute((const void*)fnet_mod_kernel, hipFuncAttributeMaxDynamicSharedMemorySize, L_BYTES);
  fnet_mod_kernel<<<256, 256, L_BYTES, stream>>>(p);
}
```

```cpp
#define PGRID 256
#define PLDS 0
#define PWG 100
#define STATL 153600
#define STAMPS
#define AMP 0
#define AMP0 0
#define AMPWG 100
#define PRE
#define POST
#define AMPPOST
#include <hip/hip_runtime.h>
#include <hip/hip_bf16.h>

typedef unsigned short u16;
typedef unsigned u32;
typedef unsigned long long u64;
typedef u32 v4u __attribute__((ext_vector_type(4)));
typedef u32 v2u __attribute__((ext_vector_type(2)));
typedef float v4f __attribute__((ext_vector_type(4)));
typedef short v4s __attribute__((ext_vector_type(4)));
typedef __bf16 v2bf __attribute__((ext_vector_type(2)));
typedef __amdgpu_buffer_rsrc_t rsrc_t;
#define LAS __attribute__((address_space(3)))
#define INL __device__ __forceinline__

struct Params {
  const u16 *x, *Cr, *Ci, *Dr, *Di, *wr, *wg, *wu, *wd;
  u16* out;
  char* ws;
};

constexpr int KB = 1024, MB = 1 << 20;
constexpr int W_EPOCH = 0, W_F1 = 32 * KB, W_F2 = 64 * KB, W_F3 = 96 * KB, W_CAND2 = 128 * KB, W_CAND4 = 160 * KB, W_DAB = 192 * KB,
              W_UPD = 224 * KB, W_DBG = 512 * KB, W_STAT1 = 1 * MB, W_STAT3 = 1 * MB + 512 * KB, W_PY2 = 2 * MB, W_PY4 = 3 * MB,
              W_XF1 = 4 * MB, W_XF3 = 6 * MB, W_ABF1 = 8 * MB, W_ABF2 = 12 * MB, W_EXC1 = 16 * MB, W_EXC2 = 25 * MB, W_END = 34 * MB;
constexpr int L_D = 0, L_RED = 64 * KB, L_STG = 64 * KB, L_WG = 112 * KB, L_WU = L_WG + 8 * KB + 128, L_WD = 129 * KB, L_MISC = 144 * KB;
constexpr int L_RS = L_MISC;
constexpr int L_TR = L_MISC + 256;
constexpr int L_H = L_TR + 4 * KB;
constexpr int L_X = L_H + 256;
constexpr int L_STAMP = L_X + KB;
constexpr int L_BYTES = L_STAMP + 512;
constexpr u32 SPIN_CAP = 1u << 17;
constexpr float EPS = 1e-5f;
constexpr int SC1 = 16;

template <int N> struct IC { static constexpr int v = N; };
template <int A, int B, class F> INL void sfor(F&& f) {
  if constexpr (A < B) { f(IC<A>{}); sfor<A + 1, B>(f); }
}

INL rsrc_t mk(const void* p, int bytes) { return __builtin_amdgcn_make_buffer_rsrc((void*)p, 0, bytes, 0x00020000); }
INL int rfl(int v) { return __builtin_amdgcn_readfirstlane(v); }
INL float rflf(float v) { return __builtin_bit_cast(float, __builtin_amdgcn_readfirstlane(__builtin_bit_cast(int, v))); }
INL v4u ld16c(rsrc_t r, int vo, int so) { return __builtin_amdgcn_raw_buffer_load_b128(r, vo, so, SC1); }
INL v4u ld16(rsrc_t r, int vo, int so) { return __builtin_amdgcn_raw_buffer_load_b128(r, vo, so, 0); }
INL void st16c(v4u v, rsrc_t r, int vo, int so) { __builtin_amdgcn_raw_buffer_store_b128(v, r, vo, so, SC1); asm volatile("s_nop 1" ::"v"(v) : "memory"); }
INL void st16(v4u v, rsrc_t r, int vo, int so) { __builtin_amdgcn_raw_buffer_store_b128(v, r, vo, so, 0); asm volatile("s_nop 1" ::"v"(v) : "memory"); }
INL float asf(u32 u) { return __builtin_bit_cast(float, u); }
INL u32 asu(float f) { return __builtin_bit_cast(u32, f); }
INL float lo16(u32 u) { return asf(u << 16); }
INL float hi16(u32 u) { return asf(u & 0xffff0000u); }
INL u32 pk(float a, float b) { v2bf p = {(__bf16)a, (__bf16)b}; return __builtin_bit_cast(u32, p); }
INL float rbf(float a) { return lo16(pk(a, a)); }
#define CLOB() asm volatile("" ::: "memory")
#define DRAIN() asm volatile("s_waitcnt vmcnt(0)" ::: "memory")
#define VMW(n) asm volatile("s_waitcnt vmcnt(%0)" ::"i"(n) : "memory")
#define LGK0() asm volatile("s_waitcnt lgkmcnt(0)" ::: "memory")
#define BAR() asm volatile("s_waitcnt lgkmcnt(0)\n\ts_barrier" ::: "memory")
INL void MFMA0(v4f& acc, const v4u& a, const v4u& b) { asm volatile("v_mfma_f32_16x16x32_bf16 %0, %1, %2, 0" : "=&v"(acc) : "v"(a), "v"(b)); }
INL void MFMA(v4f& acc, const v4u& a, const v4u& b) { asm volatile("v_mfma_f32_16x16x32_bf16 %0, %1, %2, %0" : "+v"(acc) : "v"(a), "v"(b)); }
INL void MFMA0A(v4f& acc, const v4u& a, const v4u& b) { asm volatile("v_mfma_f32_16x16x32_bf16 %0, %1, %2, 0" : "=&v"(acc) : "v"(a), "a"(b)); }
INL void MFMAA(v4f& acc, const v4u& a, const v4u& b) { asm volatile("v_mfma_f32_16x16x32_bf16 %0, %1, %2, %0" : "+v"(acc) : "v"(a), "a"(b)); }
template <class T> INL void PIN(T& x) { asm volatile("" : "+v"(x)); }
INL void PINA(v4u& x) { asm volatile("" : "+a"(x)); }
INL void VADD(float& d, float s) { asm volatile("v_add_f32 %0, %0, %1" : "+v"(d) : "v"(s)); }
INL void VADD3(float& d, float a, float b) { asm volatile("v_add_f32 %0, %1, %2" : "=v"(d) : "v"(a), "v"(b)); }

INL void dma_unit(int lds, const int (&vo)[4], rsrc_t r, int so) {
  asm volatile(
      "s_mov_b32 m0, %0\n\ts_nop 0\n\t"
      "buffer_load_dwordx4 %1, %5, %6 offen lds\n\t"
      "buffer_load_dwordx4 %2, %5, %6 offen offset:1024 lds\n\t"
      "buffer_load_dwordx4 %3, %5, %6 offen offset:2048 lds\n\t"
      "buffer_load_dwordx4 %4, %5, %6 offen offset:3072 lds"
      ::"s"(lds), "v"(vo[0]), "v"(vo[1]), "v"(vo[2]), "v"(vo[3]), "s"(r), "s"(so) : "memory");
}
INL void dma_one(int lds, int vo, rsrc_t r, int so) {
  asm volatile("s_mov_b32 m0, %0\n\ts_nop 0\n\tbuffer_load_dwordx4 %1, %2, %3 offen lds" ::"s"(lds), "v"(vo), "s"(r), "s"(so) : "memory");
}

template <int CTRL> INL u32 dppu(u32 v) { return (u32)__builtin_amdgcn_update_dpp(0, (int)v, CTRL, 0xf, 0xf, true); }
template <int CTRL> INL float dppf(float v) { return asf(dppu<CTRL>(asu(v))); }
INL u32 umax(u32 a, u32 b) { return a > b ? a : b; }
INL u32 umin(u32 a, u32 b) { return a < b ? a : b; }
INL u32 row_max(u32 v) { v = umax(v, dppu<0x128>(v)); v = umax(v, dppu<0x124>(v)); v = umax(v, dppu<0x122>(v)); return umax(v, dppu<0x121>(v)); }
INL float row_sum(float v) { v += dppf<0x128>(v); v += dppf<0x124>(v); v += dppf<0x122>(v); return v + dppf<0x121>(v); }
INL u32 max16(u32 v) { auto s = __builtin_amdgcn_permlane16_swap(v, v, false, false); return umax(s[0], s[1]); }
INL u32 max32(u32 v) { auto s = __builtin_amdgcn_permlane32_swap(v, v, false, false); return umax(s[0], s[1]); }
INL float sum16(float v) { auto s = __builtin_amdgcn_permlane16_swap(asu(v), asu(v), false, false); return asf(s[0]) + asf(s[1]); }
INL float sum32(float v) { auto s = __builtin_amdgcn_permlane32_swap(asu(v), asu(v), false, false); return asf(s[0]) + asf(s[1]); }
INL float wave_sum(float v) { return sum32(sum16(row_sum(v))); }
INL float red_g(float a, float b) { auto s = __builtin_amdgcn_permlane16_swap(asu(a), asu(b), false, false); return sum32(asf(s[0]) + asf(s[1])); }

#define STAMP(i) do { if (w == 0) { u64 t_; asm volatile("s_memrealtime %0\n\ts_waitcnt lgkmcnt(0)" : "=s"(t_)::"memory"); if (lane == 0) *(LAS u64*)(L + L_STAMP + 8 * (i)) = t_; } } while (0)

struct Sel { int t0, t1; float m0, m1, s0, s1, r0, r1; };

INL void wait_flags(rsrc_t ws, int vo, int n, int lane, u32 tlo, u32 thi) {
  for (u32 sp = 0; sp < SPIN_CAP; ++sp) {
    CLOB();
    v2u f = {tlo, thi};
    if (lane < n) f = __builtin_amdgcn_raw_buffer_load_b64(ws, vo, 0, SC1);
    if (__all(f.x == tlo && f.y == thi)) break;
  }
  asm volatile("" ::: "memory");
}
INL void gather_stats(rsrc_t ws, int so, int lane, u32 tag, float& S, float& Q, float& Dt) {
  for (u32 sp = 0; sp < SPIN_CAP; ++sp) {
    CLOB();
    v4u v[8];
#pragma unroll
    for (int i = 0; i < 8; ++i) v[i] = ld16c(ws, lane * 16, so + i * 1024);
    bool ok = true;
    S = 0.f; Q = 0.f; Dt = 0.f;
#pragma unroll
    for (int i = 0; i < 8; ++i) { ok &= v[i].w == tag; S += asf(v[i].x); Q += asf(v[i].y); Dt += asf(v[i].z); }
    if (__all(ok)) break;
  }
  S = sum32(S); Q = sum32(Q); Dt = sum32(Dt);
}
INL u32 mkkey(float r, int t) {
  u32 b = pk(r, r) & 0xffffu;
  b = (b & 0x8000u) ? (b ^ 0xffffu) : (b | 0x8000u);
  return (b << 16) | (u32)(2047 - t);
}
INL float key_r(u32 k) { u32 b = k >> 16; b = (b & 0x8000u) ? (b & 0x7fffu) : (b ^ 0xffffu); return asf(b << 16); }
INL void publish_cand(rsrc_t ws, int so, int lane, u32 tag, int tb, float r, float mean, float rs) {
  const int c = lane & 15, mt = (lane >> 4) & 1;
  const u32 key = mkkey(r, 32 * tb + 8 * (c >> 2) + 4 * mt + (c & 3));
  const u32 m1 = max16(row_max(key));
  const u32 m2 = max16(row_max(key == m1 ? 0u : key));
  if (lane < 32 && (key == m1 || key == m2)) st16c(v4u{asu(mean), asu(rs), key, tag}, ws, key == m1 ? 0 : 16, so);
}
INL Sel select_top2(rsrc_t ws, int so, int lane, u32 tag) {
  v4u a, b;
  for (u32 sp = 0; sp < SPIN_CAP; ++sp) {
    CLOB();
    a = ld16c(ws, lane * 16, so);
    b = ld16c(ws, lane * 16, so + 1024);
    if (__all(a.w == tag && b.w == tag)) break;
  }
  const u32 hi = umax(a.z, b.z), lo = umin(a.z, b.z);
  const u32 m1 = max32(max16(row_max(hi)));
  const u32 m2 = max32(max16(row_max(hi == m1 ? lo : hi)));
  const int l1 = __builtin_ctzll(__ballot(hi == m1) | (1ull << 63)), l2 = __builtin_ctzll(__ballot(hi == m2 || lo == m2) | (1ull << 63));
  const u32 mA = a.z == m1 ? a.x : b.x, sA = a.z == m1 ? a.y : b.y, mB = a.z == m2 ? a.x : b.x, sB = a.z == m2 ? a.y : b.y;
  Sel s;
  const u32 k1 = (u32)rfl((int)m1), k2 = (u32)rfl((int)m2);
  s.t0 = 2047 - (int)(k1 & 2047u); s.t1 = 2047 - (int)(k2 & 2047u);
  s.r0 = key_r(k1); s.r1 = key_r(k2);
  s.m0 = asf(__builtin_amdgcn_readlane(mA, l1)); s.s0 = asf(__builtin_amdgcn_readlane(sA, l1));
  s.m1 = asf(__builtin_amdgcn_readlane(mB, l2)); s.s1 = asf(__builtin_amdgcn_readlane(sB, l2));
  return s;
}
INL int xf_tok(int t) { const int r = t & 31; return ((t >> 5) * 2 + ((r >> 2) & 1)) * 16384 + (4 * (r >> 3) + (r & 3)) * 16; }
INL u32 tagb(float v, u32 tag, int i) { return (asu(v) & 0xffffff00u) | ((tag >> (8 * i)) & 0xffu); }
INL u32 tagdiff(v4u v, u32 tag) { return ((v.x ^ tag) | (v.y ^ (tag >> 8)) | (v.z ^ (tag >> 16)) | (v.w ^ (tag >> 24))) & 0xffu; }
INL float sigm(float r) { return rbf(1.f / (1.f + __expf(-r))); }

__global__ void __launch_bounds__(256, 1) fnet_mod_kernel(Params P) {
  extern __shared__ __attribute__((aligned(16))) char smem[];
  LAS char* L = (LAS char*)smem;
  int tid = threadIdx.x, lane = tid & 63;
  const int w = rfl(tid >> 6);
  const int b = blockIdx.x, xcd = b & 7, loc = b >> 3;
  const int k2 = xcd & 3, p2 = (xcd >> 2) * 8 + (loc >> 2), j2 = loc & 3, ec = 4 * j2 + k2;
  const int p1 = 4 * (xcd >> 1) + (loc >> 3), i1 = 8 * (xcd & 1) + (loc & 7);
  const int fline = xcd + 8 * (loc >> 3), cg = fline * 8 + (loc & 7), rho = (xcd - 4) * 8 + (loc & 7);
  const bool isGU = fline < 28;
  int c = lane & 15, g = lane >> 4, l16 = lane * 16, tok0 = 0;
  const int tb2 = 4 * p2 + ((w + j2) & 3);
  auto fresh = [&]() { PIN(l16); lane = l16 >> 4; tid = w * 64 + lane; c = lane & 15; g = lane >> 4; tok0 = 32 * tb2 + 8 * (c >> 2) + (c & 3); };
  const rsrc_t ws = mk(P.ws, W_END);
  auto stamp = [&](int i) { STAMP(i); };
  auto cstamp = [&](int i) { if (w == 0) { u64 t_; asm volatile("s_memtime %0\n\ts_waitcnt lgkmcnt(0)" : "=s"(t_)::"memory"); if (lane == 0) *(LAS u64*)(L + L_STAMP + 8 * (i)) = t_; } };
  STAMP(0);

  int vo1[4], vo4[4];
  {
    const int rowl = 8 * (lane >> 5) + ((lane >> 3) & 3), ch0 = ((lane & 7) ^ (lane >> 4)) * 16;
#pragma unroll
    for (int i = 0; i < 4; ++i) {
      const int row = rowl + 16 * (i & 1) + 4 * (i >> 1), ch = (ch0 ^ ((i & 1) * 64)) + 3072 - 1024 * i;
      vo1[i] = row * 1024 + ch; vo4[i] = row * 4096 + ch;
    }
  }
  const rsrc_t rD = mk((const char*)(i1 < 8 ? P.Dr : P.Di) + (i1 & 7) * 65536 - 3072, 65536 + 3072);
  const rsrc_t rX = mk((const char*)P.x + (128 * p1 + 32 * w) * 1024 - 3072, 32768 + 3072);
  const rsrc_t rC = mk((const char*)((k2 >> 1) ? P.Ci : P.Cr) + (size_t)p2 * 128 * 4096 + (k2 & 1) * 2048 + w * 512 - 3072, 127 * 4096 + 512 + 3072);
  const int stg = L_STG + w * 20480;
  auto dmaD = [&](int r) { const int idx = w + 4 * ((r + p1) & 3); dma_unit(L_D + idx * 4096, vo1, rD, (idx >> 3) * 32768 + (idx & 7) * 128); };
  auto dmaX = [&](int u) { dma_unit(stg + (u % 5) * 4096, vo1, rX, ((u + i1) & 7) * 128); };
  auto dmaC = [&](int n) { dma_unit(stg + ((8 + n) % 5) * 4096, vo4, rC, ((w + j2 + 1 + (n >> 2)) & 3) * 131072 + (n & 3) * 128); };
  asm volatile("s_nop 4" ::: "memory");
  dmaD(0); dmaX(0);
  const v2u ep = __builtin_amdgcn_raw_buffer_load_b64(ws, 0, W_EPOCH, SC1);
  dmaD(1); dmaD(2); dmaD(3);
  dmaX(1); dmaX(2); dmaX(3); dmaX(4);
  int A0 = c * 128 + ((g ^ (c >> 1)) * 16), A1 = A0 ^ 64;
  v4u ones = {0x3f803f80u, 0x3f803f80u, 0x3f803f80u, 0x3f803f80u};
  PIN(ones);
  VMW(16);
  asm volatile("s_barrier" ::: "memory");
  STAMP(1);
  {
    v4f r;
    const int base = L_D + (w >> 1) * 32768 + (w & 1) * 2048;
    sfor<0, 16>([&](auto I) {
      constexpr int i = I.v;
      v4u d = *(const LAS v4u*)(L + base + ((i & 1) ? A1 : A0) + (i >> 1) * 4096);
      if constexpr (i == 0) MFMA0(r, ones, d); else MFMA(r, ones, d);
    });
    asm volatile("s_nop 7" : "+v"(r));
    if (g == 0) *(LAS float*)(L + L_RS + (w * 16 + c) * 4) = r[0];
  }
  v4f acc[8], accS[2], accQ[2];
  sfor<0, 8>([&](auto U) {
    constexpr int u = U.v;
    VMW(16);
    v4u xf[4], df[8];
    const int sl = stg + (u % 5) * 4096, dk = L_D + ((u + i1) & 7) * 4096;
#pragma unroll
    for (int i = 0; i < 4; ++i) xf[i] = *(const LAS v4u*)(L + sl + ((i & 1) ? A1 : A0) + (i >> 1) * 2048);
#pragma unroll
    for (int i = 0; i < 4; ++i) PIN(xf[i]);
    LGK0();
    if constexpr (u + 5 < 8) dmaX(u + 5); else dmaC(u + 5 - 8);
#pragma unroll
    for (int i = 0; i < 8; ++i) df[i] = *(const LAS v4u*)(L + dk + ((i >> 2) ? A1 : A0) + ((i >> 1) & 1) * 32768 + (i & 1) * 2048);
    sfor<0, 2>([&](auto KB_) {
      constexpr int kb = KB_.v;
      sfor<0, 2>([&](auto MT) {
        constexpr int mt = MT.v;
        if constexpr (u == 0 && kb == 0) { MFMA0(accS[mt], xf[mt * 2 + kb], ones); MFMA0(accQ[mt], xf[mt * 2 + kb], xf[mt * 2 + kb]); }
        else { MFMA(accS[mt], xf[mt * 2 + kb], ones); MFMA(accQ[mt], xf[mt * 2 + kb], xf[mt * 2 + kb]); }
      });
      sfor<0, 8>([&](auto I) {
        constexpr int i = I.v, mt = i & 1, nn = i >> 1;
        if constexpr (u == 0 && kb == 0) MFMA0(acc[mt * 4 + nn], xf[mt * 2 + kb], df[kb * 4 + nn]);
        else MFMA(acc[mt * 4 + nn], xf[mt * 2 + kb], df[kb * 4 + nn]);
      });
    });
  });
  asm volatile("s_nop 7" : "+v"(acc[0]), "+v"(acc[1]), "+v"(acc[2]), "+v"(acc[3]), "+v"(acc[4]), "+v"(acc[5]), "+v"(acc[6]), "+v"(acc[7]),
               "+v"(accS[0]), "+v"(accS[1]), "+v"(accQ[0]), "+v"(accQ[1]));
  BAR();
  STAMP(2);
  float RS[4];
#pragma unroll
  for (int i = 0; i < 4; ++i) RS[i] = *(const LAS float*)(L + L_RS + (i * 16 + c) * 4);
  const float sgn = i1 < 8 ? 1.f : -1.f;
  auto gemm1_out = [&](const float (&mean)[8], const float (&sc)[8], int base) {
#pragma unroll
    for (int nn = 0; nn < 4; ++nn) {
      float v[8];
#pragma unroll
      for (int q = 0; q < 8; ++q) v[q] = (acc[(q >> 2) * 4 + nn][q & 3] - mean[q] * RS[nn]) * sc[q];
      st16c(v4u{pk(v[0], v[1]), pk(v[2], v[3]), pk(v[4], v[5]), pk(v[6], v[7])}, ws, l16, base + ((4 * i1 + nn) * 64 + 4 * p1 + w) * 1024);
    }
  };
  {
    float mean[8], sc[8];
#pragma unroll
    for (int q = 0; q < 8; ++q) {
      const float Q = asf(__builtin_amdgcn_ds_bpermute((20 * g + (q & 3)) * 4, asu(accQ[q >> 2][q & 3])));
      mean[q] = accS[q >> 2][q & 3] * (1.f / 512);
      sc[q] = sgn * __builtin_amdgcn_rsqf(fmaxf(Q * (1.f / 512) - mean[q] * mean[q], 0.f) + EPS);
    }
    gemm1_out(mean, sc, W_ABF1);
  }
  const u64 epoch = (((u64)(u32)rfl((int)ep.y) << 32) | (u32)rfl((int)ep.x)) + 1;
  const u32 tag = (u32)epoch, tagh = (u32)(epoch >> 32);
  {
    const v4u z = {tag - 1, tag - 1, tag - 1, tag - 1};
    if (w == 0 && lane < 40) {
      const int i = lane >> 3;
      const int off = i == 0 ? W_F2 + (p2 * 16 + ec) * 128 : i == 1 ? W_F3 + (p1 * 16 + i1) * 128 : i == 2 ? W_CAND2 + (i1 * 16 + p1) * 128
                    : i == 3 ? W_CAND4 + (ec * 16 + p2) * 128 : W_DAB + b * 128;
      st16c(z, ws, off + (lane & 7) * 16, 0);
    }
  }
  STAMP(3);
  v4u cf[64];
  DRAIN();
  asm volatile("s_barrier" ::: "memory");
  if (tid < 8) st16c(v4u{tag, tagh, tag, tagh}, ws, l16, W_F1 + (p1 * 16 + i1) * 128);
  STAMP(4);
  sfor<0, 16>([&](auto N) {
    constexpr int n = N.v;
    if constexpr (n >= 5) VMW(n + 4 < 16 ? 16 : (15 - n) * 4);
    const int sl = stg + ((8 + n) % 5) * 4096;
    sfor<0, 4>([&](auto I) {
      constexpr int i = I.v;
      cf[(n >> 2) * 16 + (i >> 1) * 8 + (n & 3) * 2 + (i & 1)] = *(const LAS v4u*)(L + sl + ((i & 1) ? A1 : A0) + (i >> 1) * 2048);
    });
    sfor<0, 4>([&](auto I) { constexpr int i = I.v; PINA(cf[(n >> 2) * 16 + (i >> 1) * 8 + (n & 3) * 2 + (i & 1)]); });
    LGK0();
    if constexpr (n + 5 < 16) dmaC(n + 5);
  });
  STAMP(5);

  auto fvo = [&]() { return ((8 * (k2 & 1) + 2 * w + ((lane >> 1) & 1)) * 16 + 8 * (k2 >> 1) + 2 * j2 + (lane & 1)) * 128; };

  auto gemm2 = [&](int abf, int exc, float (&y)[16], auto&& hook) {
    int redW[3];
#pragma unroll
    for (int u = 0; u < 3; ++u) redW[u] = L_RED + (((w + 1 + u) & 3) * 3 + u) * 4096 + l16;
    const int redR = L_RED + w * 12288 + l16;
    v4u ab[2][16];
    v4f a[2][4];
    float base[16], bx[16];
    v4u xe[9];
    auto abso = [&](int i, int f) { return abf + (((16 * (k2 >> 1) + 4 * j2 + ((k2 + 1 + i) & 3)) * 2 + (f >> 3)) * 64 + 32 * (k2 & 1) + 8 * w + (f & 7)) * 1024; };
    const int excin = exc + ((p2 * 16 + ec) * 12 + w) * 3072;
#pragma unroll
    for (int f = 0; f < 16; ++f) ab[0][f] = ld16c(ws, l16, abso(0, f));
    sfor<0, 4>([&](auto I) {
      constexpr int i = I.v;
      v4f rd[12];
      u32 pkd[8];
      sfor<0, 128>([&](auto N) {
        constexpr int n = N.v, u = n >> 5, ch = (n >> 3) & 3, ks = n & 7;
        if constexpr (ks == 0) MFMA0A(a[u & 1][ch], ab[i & 1][(ch & 1) * 8 + ks], cf[u * 16 + (ch >> 1) * 8 + ks]);
        else MFMAA(a[u & 1][ch], ab[i & 1][(ch & 1) * 8 + ks], cf[u * 16 + (ch >> 1) * 8 + ks]);
        if constexpr (i > 0 && n >= 8 && n < 24) VADD(base[n - 8], a[1][(n - 8) >> 2][(n - 8) & 3]);
        if constexpr (i > 0 && n >= 18 && n < 26) { pkd[n - 18] = pk(base[2 * (n - 18)], base[2 * (n - 18) + 1]); PIN(pkd[n - 18]); }
        if constexpr (i > 0 && n >= 26 && n < 29) {
          constexpr int q = n - 26;
          st16c(v4u{pkd[3 * q], pkd[3 * q + 1], q < 2 ? pkd[(3 * q + 2) & 7] : tag, tag}, ws, l16,
                exc + (((p2 * 16 + 4 * j2 + ((k2 + i) & 3)) * 3 + (i - 1)) * 4 + w) * 3072 + q * 1024);
        }
        if constexpr (n == 14) BAR();
        if constexpr (n >= 15 && n <= 103 && (n & 7) == 7) {
          constexpr int m = (n - 15) >> 3;
          PIN(a[(m >> 2) & 1][m & 3]);
          *(LAS v4f*)(L + redW[m >> 2] + (m & 3) * 1024) = a[(m >> 2) & 1][m & 3];
        }
        if constexpr (n == 105) BAR();
        if constexpr (n >= 105 && n < 117) rd[n - 105] = *(const LAS v4f*)(L + redR + (((n - 105) % 3) * 4 + (n - 105) / 3) * 1024);
        if constexpr (n >= 112) {
          constexpr int q = n - 112;
          VADD3(base[q], rd[(q >> 2) * 3][q & 3], rd[(q >> 2) * 3 + 1][q & 3]);
          VADD(base[q], rd[(q >> 2) * 3 + 2][q & 3]);
        }
        if constexpr (i < 3 && n >= 30 && n < 94 && (n & 3) == 2) ab[(i + 1) & 1][(n - 30) >> 2] = ld16c(ws, l16, abso(i + 1, (n - 30) >> 2));
        if constexpr (i == 3 && n >= 30 && n < 36) xe[n - 30] = ld16c(ws, l16, excin + ((n - 30) / 3) * 12288 + ((n - 30) % 3) * 1024);
        if constexpr (i == 3 && n >= 72 && n < 75) xe[n - 66] = ld16c(ws, l16, excin + 2 * 12288 + (n - 72) * 1024);
        hook(I, N);
      });
    });
    bool ok = true;
#pragma unroll
    for (int q = 0; q < 9; ++q) ok &= xe[q].w == tag;
    for (u32 sp = 0; !__all(ok) && sp < SPIN_CAP; ++sp) {
      CLOB();
      ok = true;
#pragma unroll
      for (int q = 0; q < 9; ++q) xe[q] = ld16c(ws, l16, excin + (q / 3) * 12288 + (q % 3) * 1024);
#pragma unroll
      for (int q = 0; q < 9; ++q) ok &= xe[q].w == tag;
    }
#pragma unroll
    for (int q = 0; q < 8; ++q) {
      const u32 d0 = xe[q / 3][q % 3], d1 = xe[3 + q / 3][q % 3], d2 = xe[6 + q / 3][q % 3];
      bx[2 * q] = lo16(d0) + lo16(d1) + lo16(d2);
      bx[2 * q + 1] = hi16(d0) + hi16(d1) + hi16(d2);
    }
    asm volatile("s_nop 7" : "+v"(a[1][0]), "+v"(a[1][1]), "+v"(a[1][2]), "+v"(a[1][3]));
#pragma unroll
    for (int q = 0; q < 16; ++q) y[q] = (a[1][q >> 2][q & 3] + base[q]) + bx[q];
  };
  const rsrc_t rW = mk(P.wr, 1024);
  auto tile_out = [&](const float (&v)[16], u32 (&xp)[8], int xf, int stat, bool drain_first) {
    const v4u wq = ld16(rW, g * 16, ec * 64);
#pragma unroll
    for (int q = 0; q < 8; ++q) xp[q] = pk(v[2 * q], v[2 * q + 1]);
    st16c(v4u{xp[0], xp[1], xp[2], xp[3]}, ws, l16, xf + ((tb2 * 2 + 0) * 16 + ec) * 1024);
    st16c(v4u{xp[4], xp[5], xp[6], xp[7]}, ws, l16, xf + ((tb2 * 2 + 1) * 16 + ec) * 1024);
    float s[2], q2[2], d[2];
#pragma unroll
    for (int mt = 0; mt < 2; ++mt) {
      s[mt] = 0.f; q2[mt] = 0.f; d[mt] = 0.f;
#pragma unroll
      for (int q = 0; q < 4; ++q) {
        const float a0 = lo16(xp[mt * 4 + q]), a1 = hi16(xp[mt * 4 + q]);
        s[mt] += a0 + a1; q2[mt] += a0 * a0 + a1 * a1; d[mt] += a0 * lo16(wq[q]) + a1 * hi16(wq[q]);
      }
    }
    const float S = red_g(s[0], s[1]), Q = red_g(q2[0], q2[1]), Dt = red_g(d[0], d[1]);
    if (drain_first) DRAIN();
    if (lane < 32) st16c(v4u{asu(S), asu(Q), asu(Dt), tag}, ws, l16, stat + (tb2 * 16 + ec) * 512);
  };

  fresh();
  wait_flags(ws, W_F1 + fvo(), 4, lane, tag, tagh);
  STAMP(6);
  u32 x1p[8];
  {
    float y[16];
    v4u xr[2];
    const rsrc_t rXa = mk(P.x, 2 * MB);
    const rsrc_t rG = mk((const char*)((w >> 1) ? P.wu : P.wg) + cg * 16, isGU ? 512 * 3584 - cg * 16 : 0);
    const rsrc_t rWd = mk((const char*)P.wd + cg * 8192, isGU ? 8192 : 0);
    const int pos = lane & 31;
    const int gvo = (32 * (lane >> 5) + 8 * ((pos >> 2) & 3) + 4 * (pos >> 4) + (pos & 3)) * 3584;
    gemm2(W_ABF1, W_EXC1, y, [&](auto I, auto N) {
      constexpr int i = I.v, n = N.v;
      if constexpr (i == 3 && (n == 40 || n == 41)) xr[n - 40] = ld16(rXa, (tok0 + 4 * (n - 40)) * 1024 + g * 16, ec * 64);
      if constexpr ((i == 1 || i == 2) && (n == 96 || n == 100)) {
        constexpr int q = (i - 1) * 2 + (n - 96) / 4;
        dma_one(((w >> 1) ? L_WU : L_WG) + ((w & 1) * 4 + q) * 1024, gvo, rG, ((w & 1) * 4 + q) * 64 * 3584);
      }
      if constexpr ((i == 1 || i == 2) && n == 98) dma_one(L_WD + (w * 2 + i - 1) * 1024, l16, rWd, (w * 2 + i - 1) * 1024);
    });
    STAMP(7);
    fresh();
#pragma unroll
    for (int q = 0; q < 8; ++q) { y[2 * q] += lo16(xr[q >> 2][q & 3]); y[2 * q + 1] += hi16(xr[q >> 2][q & 3]); }
    tile_out(y, x1p, W_XF1, W_STAT1, false);
  }
  DRAIN();
  asm volatile("s_barrier" ::: "memory");
  if (tid < 8) st16c(v4u{tag, tagh, tag, tagh}, ws, l16, W_F2 + (p2 * 16 + ec) * 128);
  STAMP(8);
  float Wsum;
  {
    const v4u wq = ld16(rW, l16, 0);
    float s = 0.f;
#pragma unroll
    for (int q = 0; q < 4; ++q) s += lo16(wq[q]) + hi16(wq[q]);
    Wsum = rflf(wave_sum(s));
  }
  auto wfrag = [&](int kb) {
    const int trA = ((c >> 1) & 1 ? L_WU : L_WG) + (4 * g + (c >> 2)) * 16 + (c & 1) * 8;
    const v4s lo = __builtin_amdgcn_ds_read_tr16_b64_v4i16((LAS v4s*)(L + trA + kb * 512));
    const v4s hi = __builtin_amdgcn_ds_read_tr16_b64_v4i16((LAS v4s*)(L + trA + kb * 512 + 256));
    const v2u a = __builtin_bit_cast(v2u, lo), bb = __builtin_bit_cast(v2u, hi);
    return v4u{a.x, a.y, bb.x, bb.y};
  };
  auto expert_front = [&](int cand, int xf, int py) {
    if (w == 0) {
      const Sel s = select_top2(ws, cand + (b & 15) * 2048, lane, tag);
      if (isGU) {
        v4u xr[16];
        const int vo = ((c & 1) ? xf_tok(s.t1) : xf_tok(s.t0)) + g * 256;
#pragma unroll
        for (int kb = 0; kb < 16; ++kb) xr[kb] = ld16c(ws, vo, xf + kb * 1024);
        v4f h, cs;
        ones = v4u{0x3f803f80u, 0x3f803f80u, 0x3f803f80u, 0x3f803f80u};
        PIN(ones);
        sfor<0, 16>([&](auto K) {
          constexpr int kb = K.v;
          v4u wf = wfrag(kb);
          asm volatile("s_nop 1" : "+v"(wf));
          if constexpr (kb == 0) { MFMA0(cs, wf, ones); MFMA0(h, wf, xr[kb]); } else { MFMA(cs, wf, ones); MFMA(h, wf, xr[kb]); }
        });
        asm volatile("s_nop 7" : "+v"(h), "+v"(cs));
        const float mean = (c & 1) ? s.m1 : s.m0, rs = (c & 1) ? s.s1 : s.s0;
        v4f o;
#pragma unroll
        for (int i = 0; i < 4; ++i) {
          const float v = rbf((h[i] - mean * cs[i]) * rs);
          const auto sw = __builtin_amdgcn_permlane32_swap(asu(v), asu(v), false, false);
          const float gt = asf(sw[0]), up = asf(sw[1]);
          o[i] = rbf(rbf(gt / (1.f + __expf(-gt))) * up);
        }
        if (c < 2 && g < 2) *(LAS v4f*)(L + L_H + c * 32 + g * 16) = o;
      }
      if (lane == 0) {
        *(LAS v4u*)(L + L_H + 64) = v4u{(u32)s.t0, (u32)s.t1, asu(s.m0), asu(s.m1)};
        *(LAS v4u*)(L + L_H + 80) = v4u{asu(s.s0), asu(s.s1), asu(s.r0), asu(s.r1)};
      }
    }
    u32 wd[8];
#pragma unroll
    for (int i = 0; i < 8; ++i) wd[i] = *(const LAS u32*)(L + L_WD + i * 1024 + tid * 4);
    BAR();
    if (isGU) {
      float y[4] = {0.f, 0.f, 0.f, 0.f};
#pragma unroll
      for (int q = 0; q < 4; ++q) {
        const v4f h = *(const LAS v4f*)(L + L_H + q * 16);
#pragma unroll
        for (int i = 0; i < 4; ++i) { y[(q >> 1) * 2] += h[i] * lo16(wd[(q & 1) * 4 + i]); y[(q >> 1) * 2 + 1] += h[i] * hi16(wd[(q & 1) * 4 + i]); }
      }
      st16c(v4u{tagb(y[0], tag, 0), tagb(y[1], tag, 1), tagb(y[2], tag, 2), tagb(y[3], tag, 3)}, ws, tid * 16, py + cg * 4096);
    }
    const v4u s0 = *(const LAS v4u*)(L + L_H + 64), s1 = *(const LAS v4u*)(L + L_H + 80);
    Sel s;
    s.t0 = rfl((int)s0.x); s.t1 = rfl((int)s0.y); s.m0 = rflf(asf(s0.z)); s.m1 = rflf(asf(s0.w));
    s.s0 = rflf(asf(s1.x)); s.s1 = rflf(asf(s1.y)); s.r0 = rflf(asf(s1.z)); s.r1 = rflf(asf(s1.w));
    return s;
  };
  auto expert_back = [&](int py, const Sel& s, float (&u)[4]) {
    v4f t;
    for (u32 sp = 0; sp < SPIN_CAP; ++sp) {
    CLOB();
      v4u v[7];
#pragma unroll
      for (int i = 0; i < 7; ++i) v[i] = ld16c(ws, (tid >> 3) * 4096 + (tid & 7) * 16, py + rho * 128 + i * 131072);
      u32 bad = 0;
      t = v4f{0.f, 0.f, 0.f, 0.f};
#pragma unroll
      for (int i = 0; i < 7; ++i) { bad |= tagdiff(v[i], tag); t += __builtin_bit_cast(v4f, v[i] & 0xffffff00u); }
      if (__all(bad == 0)) break;
    }
#pragma unroll
    for (int i = 0; i < 4; ++i) t[i] = sum32(sum16(t[i] + dppf<0x128>(t[i])));
    if (lane < 8) *(LAS v4f*)(L + L_X + w * 128 + l16) = t;
    BAR();
    t = v4f{0.f, 0.f, 0.f, 0.f};
#pragma unroll
    for (int i = 0; i < 4; ++i) t += *(const LAS v4f*)(L + L_X + i * 128 + (lane & 7) * 16);
    const float g0 = sigm(s.r0), g1 = sigm(s.r1);
    u[0] = rbf(rbf(t[0]) * g0); u[1] = rbf(rbf(t[1]) * g0); u[2] = rbf(rbf(t[2]) * g1); u[3] = rbf(rbf(t[3]) * g1);
  };

  DRAIN();
  if (w == 0) wait_flags(ws, W_F2 + (p1 * 16 + c) * 128, 16, lane, tag, tagh);
  asm volatile("s_barrier" ::: "memory");
  STAMP(9);
  {
    const int tb1 = 4 * p1 + w;
    A0 = c * 128 + ((g ^ (c >> 1)) * 16); A1 = A0 ^ 64;
#pragma unroll
    for (int i = 0; i < 4; ++i) RS[i] = *(const LAS float*)(L + L_RS + (i * 16 + c) * 4);
    v4u sv[8], xf[32];
#pragma unroll
    for (int i = 0; i < 8; ++i) sv[i] = ld16c(ws, l16, W_STAT1 + tb1 * 8192 + i * 1024);
#pragma unroll
    for (int i = 0; i < 32; ++i) xf[i] = ld16c(ws, l16, W_XF1 + ((tb1 * 2 + ((i >> 1) & 1)) * 16 + 2 * (((i >> 2) + i1) & 7) + (i & 1)) * 1024);
    __builtin_amdgcn_sched_barrier(0);
    float S = 0.f, Q = 0.f, Dt = 0.f;
#pragma unroll
    for (int i = 0; i < 8; ++i) { S += asf(sv[i].x); Q += asf(sv[i].y); Dt += asf(sv[i].z); }
    S = sum32(S); Q = sum32(Q); Dt = sum32(Dt);
    const float mean = S * (1.f / 512), var = fmaxf(Q * (1.f / 512) - mean * mean, 0.f), rs = __builtin_amdgcn_rsqf(var + EPS);
    publish_cand(ws, W_CAND2 + (i1 * 16 + p1) * 128 + w * 32, lane, tag, tb1, (Dt - mean * Wsum) * rs, mean, rs);
    const float c3 = (1.f + rs) * __builtin_amdgcn_rsqf(var * (1.f + rs) * (1.f + rs) + EPS);
    if (lane < 32) { *(LAS float*)(L + L_TR + w * 1024 + lane * 4) = mean; *(LAS float*)(L + L_TR + w * 1024 + 128 + lane * 4) = c3 * sgn; }
    sfor<0, 8>([&](auto U) {
      constexpr int u = U.v;
      v4u df[8];
      const int dk = L_D + ((u + i1) & 7) * 4096;
#pragma unroll
      for (int i = 0; i < 8; ++i) df[i] = *(const LAS v4u*)(L + dk + ((i >> 2) ? A1 : A0) + ((i >> 1) & 1) * 32768 + (i & 1) * 2048);
      sfor<0, 16>([&](auto I) {
        constexpr int i = I.v, kb = i >> 3, mt = i & 1, nn = (i >> 1) & 3;
        if constexpr (u == 0 && kb == 0) MFMA0(acc[mt * 4 + nn], xf[u * 4 + mt * 2 + kb], df[kb * 4 + nn]);
        else MFMA(acc[mt * 4 + nn], xf[u * 4 + mt * 2 + kb], df[kb * 4 + nn]);
      });
    });
    float mn[8], sc[8];
#pragma unroll
    for (int mt = 0; mt < 2; ++mt) {
      const v4f a = *(const LAS v4f*)(L + L_TR + w * 1024 + mt * 64 + g * 16), bb = *(const LAS v4f*)(L + L_TR + w * 1024 + 128 + mt * 64 + g * 16);
#pragma unroll
      for (int i = 0; i < 4; ++i) { mn[mt * 4 + i] = a[i]; sc[mt * 4 + i] = bb[i]; }
    }
    asm volatile("s_nop 7" : "+v"(acc[0]), "+v"(acc[1]), "+v"(acc[2]), "+v"(acc[3]), "+v"(acc[4]), "+v"(acc[5]), "+v"(acc[6]), "+v"(acc[7]));
    STAMP(10);
    gemm1_out(mn, sc, W_ABF2);
  }
  DRAIN();
  asm volatile("s_barrier" ::: "memory");
  if (tid < 8) st16c(v4u{tag, tagh, tag, tagh}, ws, l16, W_F3 + (p1 * 16 + i1) * 128);
  STAMP(11);

  const Sel s2 = expert_front(W_CAND2, W_XF1, W_PY2);
  STAMP(12);
  if (!isGU) {
    float u[4];
    expert_back(W_PY2, s2, u);
    if (tid < 8) st16c(v4u{tagb(u[0], tag, 0), tagb(u[1], tag, 1), tagb(u[2], tag, 2), tagb(u[3], tag, 3)}, ws, l16, W_UPD + rho * 128);
  }
  {
    const int n = 4 * b + w;
    const rsrc_t rDn = mk((const char*)(n < 512 ? P.Dr : P.Di) + (n & 511) * 1024, 1024);
    u32 dn[4], xa[4], xb[4];
    const int xo = (lane >> 4) * 1024 + ((lane >> 2) & 3) * 256 + (lane & 3) * 4;
#pragma unroll
    for (int i = 0; i < 4; ++i) {
      dn[i] = __builtin_amdgcn_raw_buffer_load_b32(rDn, lane * 4, i * 256, 0);
      xa[i] = __builtin_amdgcn_raw_buffer_load_b32(ws, xo, W_XF1 + xf_tok(s2.t0) + i * 4096, SC1);
      xb[i] = __builtin_amdgcn_raw_buffer_load_b32(ws, xo, W_XF1 + xf_tok(s2.t1) + i * 4096, SC1);
    }
    v4u up[4];
    for (u32 sp = 0; sp < SPIN_CAP; ++sp) {
    CLOB();
      u32 bad = 0;
#pragma unroll
      for (int i = 0; i < 4; ++i) up[i] = ld16c(ws, l16, W_UPD + i * 1024);
#pragma unroll
      for (int i = 0; i < 4; ++i) bad |= tagdiff(up[i], tag);
      if (__all(bad == 0)) break;
    }
    float dl[2], sD = 0.f;
#pragma unroll
    for (int i = 0; i < 4; ++i) sD += lo16(dn[i]) + hi16(dn[i]);
    sD = wave_sum(sD);
#pragma unroll
    for (int t = 0; t < 2; ++t) {
      const float m = t ? s2.m1 : s2.m0, rs = t ? s2.s1 : s2.s0;
      float s = 0.f, q = 0.f, dnew = 0.f, dold = 0.f;
#pragma unroll
      for (int i = 0; i < 4; ++i) {
        const u32 xx = t ? xb[i] : xa[i];
#pragma unroll
        for (int h = 0; h < 2; ++h) {
          const float x1 = h ? hi16(xx) : lo16(xx), dd = h ? hi16(dn[i]) : lo16(dn[i]);
          const float x2 = rbf(x1 + rbf(rbf((x1 - m) * rs) + hi16(up[i][2 * t + h])));
          s += x2; q += x2 * x2; dnew += x2 * dd; dold += x1 * dd;
        }
      }
      s = wave_sum(s); q = wave_sum(q); dnew = wave_sum(dnew); dold = wave_sum(dold);
      const float m2 = s * (1.f / 512), rs2 = __builtin_amdgcn_rsqf(fmaxf(q * (1.f / 512) - m2 * m2, 0.f) + EPS);
      const float var = fmaxf(1.f / (rs * rs) - EPS, 0.f), c3 = (1.f + rs) * __builtin_amdgcn_rsqf(var * (1.f + rs) * (1.f + rs) + EPS);
      dl[t] = ((dnew - m2 * sD) * rs2 - (dold - m * sD) * c3) * (n < 512 ? 1.f : -1.f);
    }
    if (lane == 0) st16c(v4u{asu(dl[0]), asu(dl[1]), tag, tag}, ws, 0, W_DAB + b * 128 + w * 16);
  }
  STAMP(13);
  float res[16];
  {
    float S, Q, Dt;
    gather_stats(ws, W_STAT1 + tb2 * 8192, lane, tag, S, Q, Dt);
    const float mean = S * (1.f / 512), rs = __builtin_amdgcn_rsqf(fmaxf(Q * (1.f / 512) - mean * mean, 0.f) + EPS);
    const auto sm = __builtin_amdgcn_permlane16_swap(asu(mean), asu(mean), false, false), sr = __builtin_amdgcn_permlane16_swap(asu(rs), asu(rs), false, false);
    const rsrc_t rCr = mk(P.Cr, 8 * MB), rCi = mk(P.Ci, 8 * MB);
    float cc[2][4];
#pragma unroll
    for (int mt = 0; mt < 2; ++mt) {
      const int ro = (tok0 + 4 * mt) * 4096;
      cc[mt][0] = lo16(__builtin_amdgcn_raw_buffer_load_b16(rCr, ro, s2.t0 * 2, 0));
      cc[mt][1] = lo16(__builtin_amdgcn_raw_buffer_load_b16(rCr, ro, s2.t1 * 2, 0));
      cc[mt][2] = lo16(__builtin_amdgcn_raw_buffer_load_b16(rCi, ro, s2.t0 * 2, 0));
      cc[mt][3] = lo16(__builtin_amdgcn_raw_buffer_load_b16(rCi, ro, s2.t1 * 2, 0));
    }
    v4u up[4];
#pragma unroll
    for (int i = 0; i < 4; ++i) up[i] = ld16c(ws, g * 64 + i * 16, W_UPD + ec * 256);
    v4u dv;
    for (u32 sp = 0; sp < SPIN_CAP; ++sp) {
    CLOB();
      dv = ld16c(ws, (lane >> 5) * 16384 + ((lane >> 2) & 7) * 128 + (lane & 3) * 16, W_DAB + ec * 1024);
      if (__all(dv.z == tag)) break;
    }
    *(LAS v2u*)(L + L_TR + w * 1024 + lane * 8) = v2u{dv.x, dv.y};
    LGK0();
#pragma unroll
    for (int q = 0; q < 4; ++q) {
      const v4f da = *(const LAS v4f*)(L + L_TR + w * 1024 + g * 64 + q * 16), db = *(const LAS v4f*)(L + L_TR + w * 1024 + 256 + g * 64 + q * 16);
#pragma unroll
      for (int mt = 0; mt < 2; ++mt) {
        const float m = asf(sm[mt]), r = asf(sr[mt]);
        const int t = tok0 + 4 * mt;
#pragma unroll
        for (int h = 0; h < 2; ++h) {
          const float x1 = h ? hi16(x1p[mt * 4 + q]) : lo16(x1p[mt * 4 + q]);
          const float u = t == s2.t0 ? hi16(up[q][h]) : t == s2.t1 ? hi16(up[q][2 + h]) : 0.f;
          const float x2 = rbf(x1 + rbf(rbf((x1 - m) * r) + u));
          res[mt * 8 + q * 2 + h] = x2 + (cc[mt][0] * da[2 * h] + cc[mt][1] * da[2 * h + 1] + cc[mt][2] * db[2 * h] + cc[mt][3] * db[2 * h + 1]);
        }
      }
    }
  }
#pragma unroll
  for (int q = 0; q < 16; ++q) PIN(res[q]);
  fresh();
  STAMP(14);
  wait_flags(ws, W_F3 + fvo(), 4, lane, tag, tagh);
  STAMP(15);
  u32 x3p[8];
  {
    float y[16];
    gemm2(W_ABF2, W_EXC2, y, [&](auto I, auto N) {
      if constexpr (N.v == 15) stamp(21 + 2 * I.v);
      if constexpr (N.v == 104) stamp(22 + 2 * I.v);
      if constexpr (N.v == 15 && I.v == 0) cstamp(29);
      if constexpr (N.v == 104 && I.v == 3) cstamp(30);
    });
    STAMP(16);
    fresh();
#pragma unroll
    for (int q = 0; q < 16; ++q) y[q] += res[q];
    tile_out(y, x3p, W_XF3, W_STAT3, true);
  }
  DRAIN();
  STAMP(17);
  v4u o[2];
  {
    float S, Q, Dt;
    gather_stats(ws, W_STAT3 + tb2 * 8192, lane, tag, S, Q, Dt);
    const float mean = S * (1.f / 512), rs = __builtin_amdgcn_rsqf(fmaxf(Q * (1.f / 512) - mean * mean, 0.f) + EPS);
    publish_cand(ws, W_CAND4 + (ec * 16 + p2) * 128 + (tb2 & 3) * 32, lane, tag, tb2, (Dt - mean * Wsum) * rs, mean, rs);
    const auto sm = __builtin_amdgcn_permlane16_swap(asu(mean), asu(mean), false, false), sr = __builtin_amdgcn_permlane16_swap(asu(rs), asu(rs), false, false);
#pragma unroll
    for (int q = 0; q < 8; ++q) {
      const float m = asf(sm[q >> 2]), r = asf(sr[q >> 2]), a0 = lo16(x3p[q]), a1 = hi16(x3p[q]);
      o[q >> 2][q & 3] = pk(a0 + rbf((a0 - m) * r), a1 + rbf((a1 - m) * r));
    }
  }
  STAMP(18);
  DRAIN();
  const Sel s4 = expert_front(W_CAND4, W_XF3, W_PY4);
  STAMP(19);
  const rsrc_t rO = mk(P.out, 2 * MB);
#pragma unroll
  for (int mt = 0; mt < 2; ++mt) {
    const int t = tok0 + 4 * mt;
    if (t != s4.t0 && t != s4.t1) st16(o[mt], rO, t * 1024 + g * 16, ec * 64);
  }
  if (b == 0 && tid == 0) __builtin_amdgcn_raw_buffer_store_b64(v2u{tag, tagh}, ws, 0, W_EPOCH, SC1);
  if (!isGU) {
    const int d = rho * 16 + (lane & 7) * 2, xo = (d >> 5) * 1024 + ((d >> 3) & 3) * 256 + (d & 7) * 2;
    const u32 xa = __builtin_amdgcn_raw_buffer_load_b32(ws, xo, W_XF3 + xf_tok(s4.t0), SC1);
    const u32 xb = __builtin_amdgcn_raw_buffer_load_b32(ws, xo, W_XF3 + xf_tok(s4.t1), SC1);
    float u[4];
    expert_back(W_PY4, s4, u);
    if (tid < 8) {
      const u32 oa = pk(lo16(xa) + rbf(rbf((lo16(xa) - s4.m0) * s4.s0) + u[0]), hi16(xa) + rbf(rbf((hi16(xa) - s4.m0) * s4.s0) + u[1]));
      const u32 ob = pk(lo16(xb) + rbf(rbf((lo16(xb) - s4.m1) * s4.s1) + u[2]), hi16(xb) + rbf(rbf((hi16(xb) - s4.m1) * s4.s1) + u[3]));
      __builtin_amdgcn_raw_buffer_store_b32(oa, rO, d * 2, s4.t0 * 1024, 0);
      __builtin_amdgcn_raw_buffer_store_b32(ob, rO, d * 2, s4.t1 * 1024, 0);
    }
  }
  DRAIN();
  STAMP(20);
  if (tid == 0) atomicMax((unsigned long long*)(P.ws + W_DBG + 8), (unsigned long long)__builtin_amdgcn_s_memrealtime());
  if (w == 0 && b == AMPWG) {
    u64 ta = *(LAS u64*)(L + L_STAMP + 8 * AMP0), tb = *(LAS u64*)(L + L_STAMP + 8 * AMP);
    const u64 end = __builtin_amdgcn_s_memrealtime() + (tb - ta) * 20;
    while (__builtin_amdgcn_s_memrealtime() < end) __builtin_amdgcn_s_sleep(2);
  }
}

__global__ void k_pre(u64* p) { if (threadIdx.x == 0) { p[0] = __builtin_amdgcn_s_memrealtime(); p[1] = 0; } }
__global__ void __launch_bounds__(256, 1) k_post(u64* p) {
  const u64 t = __builtin_amdgcn_s_memrealtime();
  __shared__ char sl[STATL];
  asm volatile("" :: "v"((LAS char*)sl));
  if (blockIdx.x == PWG) {
    const u64 end = t + (t - p[1]) * 20;
    while (__builtin_amdgcn_s_memrealtime() < end) __builtin_amdgcn_s_sleep(2);
  }
}
extern "C" void kernel_launch(void* const* d_in, const int* in_sizes, int n_in, void* d_out, int out_size, void* d_ws, size_t ws_size,
                              hipStream_t stream) {
  Params p;
  p.x = (const u16*)d_in[0]; p.Cr = (const u16*)d_in[1]; p.Ci = (const u16*)d_in[2]; p.Dr = (const u16*)d_in[3]; p.Di = (const u16*)d_in[4];
  p.wr = (const u16*)d_in[6]; p.wg = (const u16*)d_in[7]; p.wu = (const u16*)d_in[8]; p.wd = (const u16*)d_in[9];
  p.out = (u16*)d_out; p.ws = (char*)d_ws;
  (void)hipFuncSetAttribute((const void*)fnet_mod_kernel, hipFuncAttributeMaxDynamicSharedMemorySize, L_BYTES);
  k_pre<<<1, 64, 0, stream>>>((u64*)(p.ws + W_DBG));
  fnet_mod_kernel<<<256, 256, L_BYTES, stream>>>(p);
  (void)hipFuncSetAttribute((const void*)k_post, hipFuncAttributeMaxDynamicSharedMemorySize, PLDS);
  k_post<<<PGRID, 256, PLDS, stream>>>((u64*)(p.ws + W_DBG));
}
```

```cpp
#include <hip/hip_runtime.h>
#include <hip/hip_bf16.h>

typedef unsigned short u16;
typedef unsigned u32;
typedef unsigned long long u64;
typedef u32 v4u __attribute__((ext_vector_type(4)));
typedef u32 v2u __attribute__((ext_vector_type(2)));
typedef float v4f __attribute__((ext_vector_type(4)));
typedef float v2f __attribute__((ext_vector_type(2)));
typedef short v4s __attribute__((ext_vector_type(4)));
typedef __bf16 v2bf __attribute__((ext_vector_type(2)));
typedef __amdgpu_buffer_rsrc_t rsrc_t;
#define LAS __attribute__((address_space(3)))
#define INL __device__ __forceinline__

struct Params {
  const u16 *x, *Cr, *Ci, *Dr, *Di, *wr, *wg, *wu, *wd;
  u16* out;
  char* ws;
};

constexpr int KB = 1024, MB = 1 << 20;
constexpr int W_EPOCH = 0, W_F1 = 32 * KB, W_F2 = 64 * KB, W_F3 = 96 * KB, W_F4 = 128 * KB, W_CAND2 = 160 * KB, W_CAND4 = 192 * KB, W_G = 224 * KB,
              W_TOK2 = 256 * KB, W_TOK4 = 288 * KB, W_CB = 352 * KB, W_DBG = 512 * KB, W_STAT1 = 1 * MB,
              W_STAT3 = 1 * MB + 512 * KB, W_PY2 = 2 * MB, W_PY4 = 3 * MB, W_XF1 = 4 * MB, W_XF3 = 6 * MB, W_ABF1 = 8 * MB, W_ABF2 = 12 * MB,
              W_EXC1 = 16 * MB, W_EXC2 = 25 * MB, W_END = 34 * MB;
constexpr int OOB = W_END;
constexpr int L_LO = 0, L_HI = 64 * KB;
constexpr int L_STG = 64 * KB;
constexpr int L_WG = 128 * KB, L_WU = L_WG + 8 * KB + 128, L_WD = 145 * KB;
constexpr int L_XR = 128 * KB;
constexpr int L_RW = 128 * KB, L_DR = 133 * KB, L_ON = 138 * KB, L_GR = 139 * KB;
constexpr int L_ST = 153 * KB, L_H = L_ST + 512, L_X = 154 * KB, L_HP = 155 * KB;
constexpr int L_BYTES = 160 * KB;
constexpr u32 SPIN_CAP = 1u << 17;
constexpr float EPS = 1e-5f;
constexpr int SC1 = 16;
constexpr u32 ONE2 = 0x3f803f80u;
constexpr int CODE_BYTES = 36 * KB;
#define XE1 40
#define XE2 16
#define GA0 12
#define C2AT 32
#define C3AT 80
#define RSLEEP 0
#define CSLEEP 6

template <int N> struct IC { static constexpr int v = N; };
template <int A, int B, class F> INL void sfor(F&& f) {
  if constexpr (A < B) { f(IC<A>{}); sfor<A + 1, B>(f); }
}

INL rsrc_t mk(const void* p, int bytes) { return __builtin_amdgcn_make_buffer_rsrc((void*)p, 0, bytes, 0x00020000); }
INL int rfl(int v) { return __builtin_amdgcn_readfirstlane(v); }
INL float asf(u32 u) { return __builtin_bit_cast(float, u); }
INL u32 asu(float f) { return __builtin_bit_cast(u32, f); }
INL float lo16(u32 u) { return asf(u << 16); }
INL float hi16(u32 u) { return asf(u & 0xffff0000u); }
INL u32 pk(float a, float b) { v2bf p = {(__bf16)a, (__bf16)b}; return __builtin_bit_cast(u32, p); }
#define CLOB() asm volatile("" ::: "memory")
#define DRAIN() asm volatile("s_waitcnt vmcnt(0)" ::: "memory")
template <int N> INL void VMW() { asm volatile("s_waitcnt vmcnt(%0)" ::"i"(N) : "memory"); }
template <int N> INL void VMBAR() { asm volatile("s_waitcnt vmcnt(%0) lgkmcnt(0)\n\ts_barrier" ::"i"(N) : "memory"); }
#define BAR() asm volatile("s_waitcnt lgkmcnt(0)\n\ts_barrier" ::: "memory")
#define FENCE() __builtin_amdgcn_sched_barrier(0)
#define UNLIKELY(x) __builtin_expect(!!(x), 0)
INL void MFMA0(v4f& acc, const v4u& a, const v4u& b) { asm volatile("v_mfma_f32_16x16x32_bf16 %0, %1, %2, 0" : "=&v"(acc) : "v"(a), "v"(b)); }
INL void MFMA(v4f& acc, const v4u& a, const v4u& b) { asm volatile("v_mfma_f32_16x16x32_bf16 %0, %1, %2, %0" : "+v"(acc) : "v"(a), "v"(b)); }
INL void MFMA0A(v4f& acc, const v4u& a, const v4u& b) { asm volatile("v_mfma_f32_16x16x32_bf16 %0, %1, %2, 0" : "=&v"(acc) : "v"(a), "a"(b)); }
INL void MFMAA(v4f& acc, const v4u& a, const v4u& b) { asm volatile("v_mfma_f32_16x16x32_bf16 %0, %1, %2, %0" : "+v"(acc) : "v"(a), "a"(b)); }
template <class T> INL void PIN(T& x) { asm volatile("" : "+v"(x)); }
template <class T, class... R> INL void PIN(T& x, R&... r) { PIN(x); PIN(r...); }
template <class T> INL void PINS(T& x) { asm volatile("" : "+s"(x)); }
template <class T, class... R> INL void PINS(T& x, R&... r) { PINS(x); PINS(r...); }
INL void PINA(v4u& x) { asm volatile("" : "+a"(x)); }
template <int N, class T> INL void NOPS(T& x) { asm volatile("s_nop %1" : "+v"(x) : "i"(N)); }
INL void CVT(u32& d, float a, float b) { asm volatile("v_cvt_pk_bf16_f32 %0, %1, %2" : "=v"(d) : "v"(a), "v"(b)); }

#define ALD(NAME, T, OP, SC) \
  template <int O = 0> INL void NAME(T& d, rsrc_t r, int vo, int so) { asm volatile(OP " %0, %1, %2, %3 offen offset:%4" SC : "=v"(d) : "v"(vo), "s"(r), "s"(so), "i"(O) : "memory"); }
ALD(ald16c, v4u, "buffer_load_dwordx4", " sc1") ALD(ald16, v4u, "buffer_load_dwordx4", "") ALD(ald8c, v2u, "buffer_load_dwordx2", " sc1")
ALD(ald4c, u32, "buffer_load_dword", " sc1") ALD(ald4, u32, "buffer_load_dword", "") ALD(ald2, u32, "buffer_load_ushort", "")
#define AST(NAME, T, OP, SC) \
  template <int O = 0> INL void NAME(const T& d, rsrc_t r, int vo, int so) { asm volatile(OP " %0, %1, %2, %3 offen offset:%4" SC ::"v"(d), "v"(vo), "s"(r), "s"(so), "i"(O) : "memory"); }
AST(st16c, v4u, "buffer_store_dwordx4", " sc1\n\ts_nop 1") AST(st16, v4u, "buffer_store_dwordx4", "\n\ts_nop 1")
AST(st8c, v2u, "buffer_store_dwordx2", " sc1") AST(st4c, u32, "buffer_store_dword", " sc1") AST(st4, u32, "buffer_store_dword", "")
INL v4u ld16c(rsrc_t r, int vo, int so) { return __builtin_amdgcn_raw_buffer_load_b128(r, vo, so, SC1); }

INL void dma_unit(int lds, const int (&vo)[4], rsrc_t r, int so) {
  asm volatile(
      "s_mov_b32 m0, %0\n\ts_nop 0\n\t"
      "buffer_load_dwordx4 %1, %5, %6 offen lds\n\t"
      "buffer_load_dwordx4 %2, %5, %6 offen offset:1024 lds\n\t"
      "buffer_load_dwordx4 %3, %5, %6 offen offset:2048 lds\n\t"
      "buffer_load_dwordx4 %4, %5, %6 offen offset:3072 lds"
      ::"s"(lds), "v"(vo[0]), "v"(vo[1]), "v"(vo[2]), "v"(vo[3]), "s"(r), "s"(so) : "memory");
}
INL void SETM0(int lds) { asm volatile("s_mov_b32 m0, %0" ::"s"(lds) : "memory"); }
template <int O> INL void dma1(int vo, rsrc_t r, int so) { asm volatile("buffer_load_dwordx4 %0, %1, %2 offen offset:%3 lds" ::"v"(vo), "s"(r), "s"(so), "i"(O) : "memory"); }
template <int O> INL void dma1w(int vo, rsrc_t r, int so) { asm volatile("buffer_load_dword %0, %1, %2 offen offset:%3 lds" ::"v"(vo), "s"(r), "s"(so), "i"(O) : "memory"); }
template <int O> INL void dma1c(int vo, rsrc_t r, int so) { asm volatile("buffer_load_dwordx4 %0, %1, %2 offen offset:%3 sc1 lds" ::"v"(vo), "s"(r), "s"(so), "i"(O) : "memory"); }
INL void dma4c(int lds, int vo, rsrc_t r, int so) {
  asm volatile(
      "s_mov_b32 m0, %0\n\ts_nop 0\n\t"
      "buffer_load_dwordx4 %1, %2, %3 offen sc1 lds\n\t"
      "buffer_load_dwordx4 %1, %2, %3 offen offset:1024 sc1 lds\n\t"
      "buffer_load_dwordx4 %1, %2, %3 offen offset:2048 sc1 lds\n\t"
      "buffer_load_dwordx4 %1, %2, %3 offen offset:3072 sc1 lds"
      ::"s"(lds), "v"(vo), "s"(r), "s"(so) : "memory");
}

template <int CTRL> INL u32 dppu(u32 v) { return (u32)__builtin_amdgcn_update_dpp(0, (int)v, CTRL, 0xf, 0xf, true); }
template <int CTRL> INL float dppf(float v) { return asf(dppu<CTRL>(asu(v))); }
INL u32 umax(u32 a, u32 b) { return a > b ? a : b; }
INL u32 umin(u32 a, u32 b) { return a < b ? a : b; }
INL float row_sum(float v) { v += dppf<0x128>(v); v += dppf<0x124>(v); v += dppf<0x122>(v); return v + dppf<0x121>(v); }
INL float sum16(float v) { auto s = __builtin_amdgcn_permlane16_swap(asu(v), asu(v), false, false); return asf(s[0]) + asf(s[1]); }
INL float sum32(float v) { auto s = __builtin_amdgcn_permlane32_swap(asu(v), asu(v), false, false); return asf(s[0]) + asf(s[1]); }
INL float sel4(const v4f& v, int i) { const float a = (i & 1) ? v[1] : v[0], b = (i & 1) ? v[3] : v[2]; return (i & 2) ? b : a; }
template <int CTRL> INL void top2_dpp(u32& hi, u32& lo) { const u32 t = umin(hi, dppu<CTRL>(hi)), l = umax(lo, dppu<CTRL>(lo)); hi = umax(hi, dppu<CTRL>(hi)); lo = umax(t, l); }
INL void top2_row(u32& hi, u32& lo) { top2_dpp<0x128>(hi, lo); top2_dpp<0x124>(hi, lo); top2_dpp<0x122>(hi, lo); top2_dpp<0x121>(hi, lo); }
INL void top2_16(u32& hi, u32& lo) {
  const auto h = __builtin_amdgcn_permlane16_swap(hi, hi, false, false), l = __builtin_amdgcn_permlane16_swap(lo, lo, false, false);
  lo = umax(umin(h[0], h[1]), umax(l[0], l[1])); hi = umax(h[0], h[1]);
}
INL void top2_32(u32& hi, u32& lo) {
  const auto h = __builtin_amdgcn_permlane32_swap(hi, hi, false, false), l = __builtin_amdgcn_permlane32_swap(lo, lo, false, false);
  lo = umax(umin(h[0], h[1]), umax(l[0], l[1])); hi = umax(h[0], h[1]);
}
INL u32 differ(u32 acc, u32 a, u32 t) { return __builtin_amdgcn_bitop3_b32(acc, a, t, 0xf6); }

#define STAMP(i)

INL void check_flags(v2u f, const v2u& ft, rsrc_t ws, int vo, int so) {
  if (UNLIKELY(__any(differ(f.x ^ ft.x, f.y, ft.y) != 0))) {
    for (u32 sp = 0; sp < SPIN_CAP; ++sp) {
      CLOB();
      f = __builtin_amdgcn_raw_buffer_load_b64(ws, vo, so, SC1);
      if (!__any(differ(f.x ^ ft.x, f.y, ft.y) != 0)) break;
    }
  }
  CLOB();
}
template <int N, int W = 3, class LD> INL void resettle(u32 bad, v4u* v, u32 tag, LD&& ld) {
  if (UNLIKELY(__any(bad != 0))) {
    for (u32 sp = 0; sp < SPIN_CAP; ++sp) {
      CLOB();
      bad = 0;
#pragma unroll
      for (int q = 0; q < N; ++q) v[q] = ld(q);
#pragma unroll
      for (int q = 0; q < N; ++q) bad = differ(bad, v[q][W], tag);
      if (!__any(bad != 0)) break;
    }
  }
}
template <int N, int W = 3, class LD> INL void settle(v4u* v, u32 tag, LD&& ld) {
  u32 bad = v[0][W] ^ tag;
#pragma unroll
  for (int q = 1; q < N; ++q) bad = differ(bad, v[q][W], tag);
  resettle<N, W>(bad, v, tag, ld);
}
INL u32 mkkey(float r, int slot) {
  const u32 b = pk(r, r);
  return ((b ^ (((int)b >> 31) | 0x80000000u)) & 0xffff0000u) | (u32)slot;
}
INL float key_r(u32 k) { return asf((k ^ ((~(int)k >> 31) | 0x80000000u)) & 0xffff0000u); }
INL int slot_tok(int s) { return (s & ~31) + 8 * ((s >> 2) & 3) + 4 * ((s >> 4) & 1) + (s & 3); }
INL int slot_xf(u32 k) { return (k & 0x7f0u) * 1024 + (k & 15u) * 16; }
INL float sigm(float r) { return __builtin_amdgcn_rcpf(1.f + __expf(-r)); }

template <int NF, bool NEXT, class RD, class NX, class MM, class SY, class HK>
INL void pass(v4u (&R)[8], RD&& rd, NX&& nx, MM&& mm, SY&& sync, HK&& hook) {
  sfor<0, 2 * NF>([&](auto N) {
    constexpr int n = N.v, f = n >> 1;
    if constexpr ((n & 3) == 0) PIN(R[(f + 1) & 7]);
    mm(IC<f>{}, IC<(n & 1)>{}, R[f & 7]);
    FENCE();
    if constexpr ((n & 1) && f + 8 < NF) R[f & 7] = rd(IC<f + 8>{});
    if constexpr (NEXT) {
      constexpr int e = 2 * NF - n;
      if constexpr (e == 9) { sync(); R[0] = nx(IC<0>{}); R[1] = nx(IC<1>{}); }
      if constexpr (e <= 8 && e >= 5) R[10 - e] = nx(IC<10 - e>{});
      if constexpr (e == 3) R[6] = nx(IC<6>{});
      if constexpr (e == 1) R[7] = nx(IC<7>{});
    }
    hook(N);
    FENCE();
  });
}

__global__ void __launch_bounds__(256, 1) fnet_mod_kernel(Params P) {
  extern __shared__ __attribute__((aligned(16))) char smem[];
  LAS char* L = (LAS char*)smem;
  int lane = threadIdx.x & 63;
  const int b = blockIdx.x, xcd = b & 7, loc = b >> 3;
  u32 touch;
  {
    u64 pc;
    asm volatile("s_getpc_b64 %0" : "=s"(pc));
    ald4(touch, mk((const void*)(pc & ~127ull), CODE_BYTES), (lane < 16 ? lane : 16 + ((int)(threadIdx.x >> 6) + loc * 4) * 3 + (lane & 3)) * 128, 0);
  }
  const int w = rfl(threadIdx.x >> 6);
  const int k2 = xcd & 3, p2 = (xcd >> 2) * 8 + (loc >> 2), j2 = loc & 3, ec = 4 * j2 + k2, tb2 = 4 * p2 + w;
  const int p1 = 8 * (xcd >> 1) + (loc >> 2), i1 = 4 * (xcd & 1) + (loc & 3);
  const int fline = xcd + 8 * (loc >> 3), cg = fline * 8 + (loc & 7), rho = ((xcd - 4) * 8 + (loc & 7)) & 31;
  const bool isGU = fline < 28;
  int c = lane & 15, g = lane >> 4, l16 = lane * 16;
  auto fresh = [&]() { PIN(l16); lane = l16 >> 4; c = lane & 15; g = lane >> 4; };
  const rsrc_t ws = mk(P.ws, W_END);
  STAMP(0);

  int vo1[4], vo4[4];
  {
    const int rowl = 8 * (lane >> 5) + ((lane >> 3) & 3), ch0 = ((lane & 7) ^ (lane >> 4)) * 16;
#pragma unroll
    for (int i = 0; i < 4; ++i) {
      const int row = rowl + 16 * (i & 1) + 4 * (i >> 1), ch = (ch0 ^ ((i & 1) * 64)) + 3072 - 1024 * i;
      vo1[i] = row * 1024 + ch; vo4[i] = row * 4096 + ch;
    }
  }
  const rsrc_t rX = mk((const char*)P.x - 3072, 2 * MB + 3072);
  const rsrc_t rD = mk((const char*)(i1 < 4 ? P.Dr : P.Di) + ((i1 & 3) * 4 + w) * 32768 - 3072, 32768 + 3072);
  const rsrc_t rC = mk((const char*)((k2 >> 1) ? P.Ci : P.Cr) + (size_t)tb2 * 131072 + (k2 & 1) * 2048 - 3072, 31 * 4096 + 2048 + 3072);
  const int stg = L_STG + w * 20480;
  auto posD = [](int u) { return u; };
  auto posC = [](int n) { return n + 8; };
  auto dmaX = [&](int r) { const int rr = (r + i1) & 3; dma_unit(L_LO + rr * 16384 + w * 4096, vo1, rX, (2 * p1 + (w & 1)) * 32768 + (2 * ((rr + p1) & 3) + (w >> 1)) * 128); };
  auto dmaD = [&](int u) { dma_unit(stg + (posD(u) % 5) * 4096, vo1, rD, ((u + 2 * p1) & 7) * 128); };
  auto dmaC = [&](int n) { dma_unit(stg + (posC(n) % 5) * 4096, vo4, rC, ((n + 4 * j2) & 15) * 128); };
  auto oneD = [&](int u, auto I) { if constexpr (I.v == 0) { SETM0(stg + (posD(u) % 5) * 4096); asm volatile("s_nop 0"); } dma1<I.v * 1024>(vo1[I.v], rD, ((u + 2 * p1) & 7) * 128); };
  auto oneC = [&](int n, auto I) { if constexpr (I.v == 0) { SETM0(stg + (posC(n) % 5) * 4096); asm volatile("s_nop 0"); } dma1<I.v * 1024>(vo4[I.v], rC, ((n + 4 * j2) & 15) * 128); };
  asm volatile("s_nop 4" ::: "memory");
  dmaX(0);
  STAMP(32);
  dmaX(1); dmaX(2); dmaX(3);
  v2u ep;
  v4u wv;
  const rsrc_t rW = mk(P.wr, 1024);
  asm volatile("buffer_load_dwordx2 %0, off, %1, 0 sc1" : "=v"(ep) : "s"(ws) : "memory");
  ald16(wv, rW, l16, 0);
  dmaD(0); dmaD(1);
  STAMP(33);
  int A0 = c * 128 + ((g ^ (c >> 1)) * 16), A1 = A0 ^ 64;
  int sa0 = stg + A0, sa1 = stg + A1, xs0 = A0 + w * 2048, xs1 = xs0 ^ 64, sta = L_ST + g * 16;
  v4u ones = {ONE2, ONE2, ONE2, ONE2};
  PIN(A0, A1, sa0, sa1, xs0, xs1, sta, ones);
  v4u dfr[32], cf[64], R[8];
  v4f acc[8];
  auto readD = [&](auto U) {
    sfor<0, 4>([&](auto I) { constexpr int i = I.v; dfr[U.v * 4 + i] = *(const LAS v4u*)(L + ((i >> 1) ? sa1 : sa0) + (posD(U.v) % 5) * 4096 + (i & 1) * 2048); });
  };
  auto readC = [&](auto N) {
    sfor<0, 4>([&](auto I) { constexpr int i = I.v; cf[N.v * 4 + i] = *(const LAS v4u*)(L + ((i >> 1) ? sa1 : sa0) + (posC(N.v) % 5) * 4096 + (i & 1) * 2048); });
  };
  auto pinD = [&](auto U) { sfor<0, 4>([&](auto I) { PIN(dfr[U.v * 4 + 3 - I.v]); }); };
  auto pinC = [&](auto N) { sfor<0, 4>([&](auto I) { PINA(cf[N.v * 4 + 3 - I.v]); }); };
  const float sgn = asf(0x3f800000u | ((u32)(i1 & 4) << 29));
  asm volatile("" ::"s"(rC), "v"(vo4[0]), "v"(vo4[1]), "v"(vo4[2]), "v"(vo4[3]));
  VMBAR<8>();
  PIN(ep, touch, wv);
  STAMP(1);
  v4f accR[2], mn[4], sc[4], S, Q;
  auto rdx1 = [&](auto F) { constexpr int f = F.v; return *(const LAS v4u*)(L + ((f & 4) ? A1 : A0) + (f >> 3) * 8192 + (f & 3) * 2048); };
  {
    v4u xs[16];
    sfor<0, 16>([&](auto I) { xs[I.v] = *(const LAS v4u*)(L + ((I.v & 1) ? xs1 : xs0) + (I.v >> 1) * 8192); CLOB(); });
    sfor<0, 16>([&](auto I) {
      constexpr int i = I.v;
      if constexpr (i == 0) { MFMA0(S, xs[0], ones); MFMA0(Q, xs[0], xs[0]); } else { MFMA(S, xs[i], ones); MFMA(Q, xs[i], xs[i]); }
      if constexpr (i % 3 == 0 && i < 12) oneD(2, IC<i / 3>{});
    });
  }
  STAMP(34);
  dmaD(3);
  sfor<0, 6>([&](auto U) {
    constexpr int u = U.v;
    VMW<12>(); readD(U);
    if constexpr (u > 0) pinD(IC<u - 1>{});
    if constexpr (u < 4) dmaD(u + 4); else dmaC(u - 4);
  });
  STAMP(35);
  sfor<0, 8>([&](auto F) { R[F.v] = rdx1(F); });
  u32 tag;
  v2u ft;
  float Wsum;
  int abo = (4 * i1 + w) * 131072 + p1 * 4096;
  {
    NOPS<0>(Q); PIN(S);
    const float mean = sel4(S, c) * (1.f / 512);
    const float rs = sgn * __builtin_amdgcn_rsqf(fmaxf(sel4(Q, c) * (1.f / 512) - mean * mean, 0.f) + EPS);
    if ((c >> 2) == g) { *(LAS float*)(L + L_ST + w * 128 + c * 4) = mean; *(LAS float*)(L + L_ST + w * 128 + 64 + c * 4) = rs; }
  }
  pass<64, false>(R, rdx1, 0,
    [&](auto F, auto H, const v4u& fr) {
      constexpr int f = F.v, h = H.v;
      if constexpr (f < 4) MFMA0(acc[(f & 3) * 2 + h], fr, dfr[(f >> 2) * 2 + h]); else MFMA(acc[(f & 3) * 2 + h], fr, dfr[(f >> 2) * 2 + h]);
      if constexpr ((f & 3) == 0) { if constexpr (f == 0) MFMA0(accR[h], ones, dfr[h]); else MFMA(accR[h], ones, dfr[(f >> 2) * 2 + h]); }
    }, 0,
    [&](auto N) {
      constexpr int n = N.v, u = n >> 4, m = n & 15;
      if constexpr (n == 10) { VMW<8>(); readD(IC<6>{}); }
      if constexpr (n == 14) readD(IC<7>{});
      if constexpr (n == 30) { pinD(IC<5>{}); pinD(IC<6>{}); BAR(); }
      if constexpr (n >= C2AT && n < C2AT + 32 && (n - C2AT) % 8 == 0) oneC(2, IC<(n - C2AT) / 8>{});
      if constexpr (n >= C3AT && n < C3AT + 32 && (n - C3AT) % 8 == 0) oneC(3, IC<(n - C3AT) / 8>{});
      if constexpr (u == 7 && m >= 4 && m < 12) { if constexpr (m & 1) sc[(m - 4) >> 1] = *(const LAS v4f*)(L + sta + 64 + ((m - 4) >> 1) * 128); else mn[(m - 4) >> 1] = *(const LAS v4f*)(L + sta + ((m - 4) >> 1) * 128); }
    });
  STAMP(2);
  float RS[2];
  NOPS<7>(acc[7]); PIN(accR[0], accR[1]);
  asm volatile("v_mov_b32 %0, %2\n\tv_mov_b32 %1, %3" : "=&v"(RS[0]), "=&v"(RS[1]) : "v"(accR[0][0]), "v"(accR[1][0]));
  auto gemm1_out = [&](auto CENTRED, int so) {
    sfor<0, 8>([&](auto I) { PIN(acc[I.v]); });
    sfor<0, 4>([&](auto Qq) {
      constexpr int q = Qq.v, t0 = q & 2, me = q & 1;
      v4f a = acc[t0 * 2 + me], bb = acc[t0 * 2 + 2 + me];
      if constexpr (!CENTRED.v) { a -= mn[t0] * RS[me]; bb -= mn[t0 + 1] * RS[me]; }
      a *= sc[t0]; bb *= sc[t0 + 1];
      st16c<q * 1024>(v4u{pk(a[0], a[1]), pk(a[2], a[3]), pk(bb[0], bb[1]), pk(bb[2], bb[3])}, ws, l16, so);
    });
  };
  gemm1_out(IC<0>{}, W_ABF1 + abo);
  STAMP(3);
  v4u I0, I1, wq;
  int fvo, pvo, hi_a, evo, eio;
  v2u pv, fl;
  int tbase[4], qoff[4], xout[3];
  int xin = W_EXC1 + ((p2 * 16 + ec) * 12 + w) * 3072, w4k = w * 4096;
  auto consts2 = [&]() {
    const u32 one = (c >> 2) == g ? (0x3f80u << (16 * (c & 1))) : 0u, d0 = (c & 2) ? 0u : one, d1 = (c & 2) ? one : 0u;
    I0 = v4u{d0, d1, 0u, 0u}; I1 = v4u{0u, 0u, d0, d1};
    hi_a = L_HI + l16; eio = (lane & 1) * 12288; evo = lane < 32 ? (g ? xout[1] : xout[0]) + c * 8 : OOB;
    fvo = ((16 * (k2 & 1) + 4 * (lane & 3) + w) * 8 + 4 * (k2 >> 1) + j2) * 128;
    PIN(I0, I1, hi_a, eio, evo, fvo);
  };
  const rsrc_t rWd = mk((const char*)P.wd + cg * 8192, isGU ? 8192 : 0);
  const rsrc_t rG = mk((const char*)((w >> 1) ? P.wu : P.wg) + (cg >> 3) * 128, isGU ? 512 * 3584 - (cg >> 3) * 128 : 0);
  int gvo = ((w & 1) * 256 + 32 * (lane >> 5) + 8 * ((lane >> 2) & 3) + 4 * ((lane >> 4) & 1) + (lane & 3)) * 3584 + (cg & 7) * 16;
  int tvo = (((cg & 7) * 2 + (w & 1)) * 32 + (lane & 31)) * 3584, xra = L_XR + w4k + (A0 ^ ((ec & 1) * 64)), g16 = g * 16;
  int xrs = tb2 * 32768 + (ec >> 1) * 128, gm0 = ((w >> 1) ? L_WU : L_WG) + (w & 1) * 4096;
  {
    const u64 epoch = (((u64)(u32)rfl((int)ep.y) << 32) | (u32)rfl((int)ep.x)) + 1;
    tag = (u32)epoch & 0xbfffbfffu;
    ft = v2u{(u32)epoch, (u32)(epoch >> 32)};
    pvo = w == 0 ? (W_F1 + g * 32 * KB + ((g & 1) ? p2 * 16 + ec : p1 * 8 + i1) * 128 + c * 8) : (w < 3 && ec == p2) ? W_CAND4 + ((w - 1) * 4 + g) * 2048 + p2 * 128 + c * 8 : OOB;
    pv = (w == 0 && g == 0) ? ft : v2u{ft.x ^ 1u, ft.x ^ 1u};
    PIN(ft, pvo, pv); PINS(tag);
    VMW<12>(); readC(IC<0>{}); readC(IC<1>{});
    dmaC(4);
    float t = 0.f;
#pragma unroll
    for (int q = 0; q < 4; ++q) t += lo16(wv[q]) + hi16(wv[q]);
    Wsum = asf((u32)rfl((int)asu(sum32(sum16(row_sum(t))))));
#pragma unroll
    for (int i = 0; i < 4; ++i) { tbase[i] = (16 * (k2 >> 1) + 4 * j2 + ((k2 + 1 + i) & 3)) * 131072 + W_ABF1; qoff[i] = (32 * (k2 & 1) + 8 * ((i + j2) & 3) + 2 * w) * 2048; PINS(tbase[i], qoff[i]); }
#pragma unroll
    for (int i = 0; i < 3; ++i) { xout[i] = W_EXC1 + (((p2 * 16 + 4 * j2 + ((k2 + 1 + i) & 3)) * 3 + i) * 4 + w) * 3072; PINS(xout[i]); }
    consts2();
    PINS(Wsum, xin, w4k, xrs, gm0); PIN(gvo, tvo, xra, g16);
    asm volatile("" ::"s"(rWd), "s"(rG));
  }
  VMW<12>(); readC(IC<2>{}); pinC(IC<0>{});
  dmaC(5);
  VMW<12>(); readC(IC<3>{}); pinC(IC<1>{});
  dmaC(6);
  VMBAR<12>();
  st8c(pv, ws, pvo, 0);
  STAMP(4);
  pinC(IC<2>{}); dmaC(7);
  VMW<13>(); readC(IC<4>{}); pinC(IC<3>{}); dmaC(8);
  VMW<13>(); readC(IC<5>{}); pinC(IC<4>{}); dmaC(9);
  ald8c(fl, ws, fvo, W_F1);
  VMW<14>(); readC(IC<6>{}); pinC(IC<5>{}); dmaC(10);
  VMW<13>(); readC(IC<7>{}); pinC(IC<6>{}); dmaC(11);
  VMW<13>(); readC(IC<8>{}); pinC(IC<7>{}); dmaC(12);
  VMW<12>(); readC(IC<9>{});
  PIN(fl);
  check_flags(fl, ft, ws, fvo, W_F1);
  STAMP(5);

  u32 pd[8];
  v4u xe[7];
  v4f own[4];
  auto rdt = [&](auto S_, auto F) { return *(const LAS v4u*)(L + ((S_.v & 1) ? hi_a : l16) + F.v * 1024); };
  auto img = [&](int s, int r) { return ((s & 1) ? L_HI : L_LO) + r * 16384 + w4k; };
  auto mm2 = [&](auto S_, auto F, auto H, const v4u& fr) {
    constexpr int s = S_.v, f = F.v;
    v4f& a = s == 3 ? own[H.v * 2 + (f & 1)] : acc[(s & 1) * 4 + H.v * 2 + (f & 1)];
    if constexpr (f < 2 && s < 3) MFMA0A(a, fr, cf[(f >> 1) * 2 + H.v]); else MFMAA(a, fr, cf[(f >> 1) * 2 + H.v]);
  };
#define HPHASE 0
  int phase = 2;
  auto half = [&](auto Hh, auto VM, auto&& hook) {
    constexpr int h = Hh.v, s = h >> 1, f0 = 32 * (h & 1);
    pass<32, (h < 7)>(R, [&](auto F) { return rdt(IC<s>{}, IC<f0 + F.v>{}); }, [&](auto F) { return rdt(IC<((h + 1) >> 1)>{}, IC<32 - f0 + F.v>{}); },
      [&](auto F, auto H, const v4u& fr) { mm2(IC<s>{}, IC<f0 + F.v>{}, H, fr); },
      [&]() { VMBAR<VM.v>(); if (phase == HPHASE) STAMP(45 + h); }, hook);
  };
  int fso;
  auto feed = [&](auto N, auto N0, auto S_, auto Rr, auto NG) {
    constexpr int m = N.v - N0.v;
    if constexpr (m >= 0 && m < 16 * NG.v) {
      if constexpr (m % 16 == 0) { SETM0(img(S_.v, Rr.v + m / 16)); fso = tbase[S_.v] + qoff[Rr.v + m / 16]; PINS(fso); }
      if constexpr (m % 4 == 2) dma1c<((m / 4) & 3) * 1024>(l16, ws, fso);
    }
  };
  auto exch = [&](auto Hh, auto N, auto FV) {
    constexpr int h = Hh.v, n = N.v;
    if constexpr ((h == 2 || h == 4) && n >= 6 && n < 18 && n != 11 && n != 13) {
      constexpr int m = n < 11 ? n - 6 : n == 12 ? 5 : n - 8, mt = m / 5, q = m % 5;
      v4f* a = &acc[h == 2 ? 0 : 4];
      if constexpr (q < 4) CVT(pd[mt * 4 + q], a[mt * 2 + (q >> 1)][(q & 1) * 2], a[mt * 2 + (q >> 1)][(q & 1) * 2 + 1]);
      else st16c<mt * 1024>(v4u{pd[mt * 4], pd[mt * 4 + 1], pd[mt * 4 + 2], pd[mt * 4 + 3]}, ws, l16, xout[h / 2 - 1]);
    }
    if constexpr (h == 5 && n == 8) { VMW<FV.v>(); st8c<2048>(ft, ws, evo, 0); }
    if constexpr (h == 5 && n == 54) ald8c<2048>(fl, ws, eio, xin);
    if constexpr (h == 6 && n >= 6 && n < 28 && (n & 1) == 0) {
      constexpr int m = (n - 6) / 2;
      if constexpr (m != 3 && m != 7 && m != 10) { constexpr int q = m < 2 ? m : m == 2 ? 6 : m < 6 ? m - 2 : m == 6 ? 7 : m - 4; CVT(pd[q], acc[q >> 1][(q & 1) * 2], acc[q >> 1][(q & 1) * 2 + 1]); }
      if constexpr (m == 3) st16c(v4u{pd[0], pd[1], pd[6], tag}, ws, l16, xout[2]);
      if constexpr (m == 7) st16c<1024>(v4u{pd[2], pd[3], pd[7], tag}, ws, l16, xout[2]);
      if constexpr (m == 10) st16c<2048>(v4u{pd[4], pd[5], tag, tag}, ws, l16, xout[2]);
    }
    if constexpr (h == 6 && n == XE1) { VMW<3>(); PIN(fl); check_flags(fl, ft, ws, eio + 2048, xin); }
    if constexpr (h == 6 && n > XE1 && n <= XE1 + 8 && (n & 1) == 0) ald16c<(((n - XE1 - 2) / 2) & 1) * 1024>(xe[(n - XE1 - 2) / 2], ws, l16, xin + ((n - XE1 - 2) / 4) * 12288);
    if constexpr (h == 7 && n >= XE2 && n < XE2 + 6 && (n & 1) == 0) ald16c<((n - XE2) / 2) * 1024>(xe[4 + (n - XE2) / 2], ws, l16, xin + 2 * 12288);
  };
  auto finish = [&](v4u (&xp)[2], int xf, int stat) {
    int xo = xf + tb2 * 32768 + ec * 1024, so = stat + (tb2 * 16 + ec) * 512, svo = (c >> 2) == g ? c * 16 : OOB;
    PIN(xe[0], xe[1], xe[2], xe[3]);
    sfor<0, 8>([&](auto Qq) { constexpr int q = Qq.v; MFMA(own[q & 3], (q & 1) ? I1 : I0, xe[q >> 1]); });
    PINS(xo, so); PIN(svo);
    DRAIN();
    PIN(xe[4], xe[5], xe[6]);
    settle<3>(&xe[4], tag, [&](int q) { return ld16c(ws, l16 + q * 1024, xin + 2 * 12288); });
    v4u t0 = {xe[4].z, xe[5].z, xe[4].z, xe[5].z};
    PIN(t0);
    MFMA(own[0], I0, xe[4]); MFMA(own[1], I0, xe[5]); MFMA(own[2], I0, xe[6]); MFMA(own[3], I0, t0);
    STAMP(phase == 2 ? 7 : 16);
    NOPS<5>(own[1]); PIN(own[0]);
    v4f s[2], q[2];
    xp[0] = v4u{pk(own[0][0], own[0][1]), pk(own[0][2], own[0][3]), pk(own[1][0], own[1][1]), pk(own[1][2], own[1][3])};
    st16c(xp[0], ws, l16, xo);
    MFMA0(s[0], wq, xp[0]); MFMA0(q[0], xp[0], xp[0]);
    PIN(own[2], own[3]);
    xp[1] = v4u{pk(own[2][0], own[2][1]), pk(own[2][2], own[2][3]), pk(own[3][0], own[3][1]), pk(own[3][2], own[3][3])};
    st16c(xp[1], ws, l16, xo + 16384);
    MFMA0(s[1], wq, xp[1]); MFMA0(q[1], xp[1], xp[1]);
    PIN(s[0], q[0]);
    st16c(v4u{asu(s[0][0]), asu(s[0][1]), asu(sel4(q[0], c)), tag}, ws, svo, so);
    NOPS<0>(q[1]); PIN(s[1]);
    st16c<256>(v4u{asu(s[1][0]), asu(s[1][1]), asu(sel4(q[1], c)), tag}, ws, svo, so);
  };

  v4u x1p[2];
  {
    dma4c(img(0, 0), l16, ws, tbase[0] + qoff[0]);
    dma4c(img(0, 1), l16, ws, tbase[0] + qoff[1]);
    VMW<16>(); readC(IC<10>{}); pinC(IC<8>{}); dmaC(13);
    VMW<16>(); readC(IC<11>{}); pinC(IC<9>{}); dmaC(14);
    VMW<16>(); readC(IC<12>{}); pinC(IC<10>{}); dmaC(15);
    dma4c(img(0, 2), l16, ws, tbase[0] + qoff[2]);
    VMW<12>(); readC(IC<13>{});
    dma4c(img(0, 3), l16, ws, tbase[0] + qoff[3]);
    VMW<12>(); readC(IC<14>{});
    VMW<8>(); readC(IC<15>{});
    BAR();
    STAMP(6);
    sfor<0, 8>([&](auto F) { R[F.v] = rdt(IC<0>{}, F); });
    sfor<11, 16>([&](auto N) { pinC(N); });
    u32 tch;
    v4u xr[2];
    auto gather = [&](auto N, auto N0, auto Qq) {
      if constexpr (N.v == N0.v) SETM0(gm0);
      if constexpr (N.v == N0.v + 2) dma1<Qq.v * 1024>(gvo, rG, Qq.v * (64 * 3584 - 1024));
    };
    half(IC<0>{}, IC<14>{}, [&](auto N) {
      constexpr int n = N.v;
      feed(N, IC<0>{}, IC<1>{}, IC<0>{}, IC<2>{});
      if constexpr (n == 32) SETM0(L_XR + w4k);
      if constexpr (n > 32 && n < 48 && n % 4 == 2) dma1<(n - 34) / 4 * 1024>(vo1[(n - 34) / 4], rX, xrs);
      if constexpr (n == 50) ald16(wq, rW, g16, ec * 64);
      if constexpr (n == 52) ald4(tch, rG, tvo, 0);
    });
    half(IC<1>{}, IC<11>{}, [&](auto N) {
      constexpr int n = N.v;
      feed(N, IC<0>{}, IC<1>{}, IC<2>{}, IC<2>{});
      if constexpr (n == 32) SETM0(L_WD + w * 2048);
      if constexpr (n == 34) dma1<0>(l16, rWd, w * 2048);
      if constexpr (n == 38) dma1<1024>(l16, rWd, w * 2048);
      if constexpr (n == 44) { VMW<11>(); PIN(wq); xr[0] = *(const LAS v4u*)(L + xra); }
      if constexpr (n == 46) xr[1] = *(const LAS v4u*)(L + xra + 2048);
    });
    half(IC<2>{}, IC<13>{}, [&](auto N) {
      exch(IC<2>{}, N, IC<0>{});
      feed(N, IC<18>{}, IC<2>{}, IC<0>{}, IC<2>{});
      gather(N, IC<50>{}, IC<0>{});
    });
    half(IC<3>{}, IC<10>{}, [&](auto N) {
      constexpr int n = N.v;
      if constexpr (n == 0) PIN(tch);
      if constexpr (n < 32 && (n & 1) == 0) {
        constexpr int q = n / 8, i = (n / 2) & 3;
        float e = (i & 1) ? hi16(xr[q >> 1][(q & 1) * 2 + (i >> 1)]) : lo16(xr[q >> 1][(q & 1) * 2 + (i >> 1)]);
        PIN(e);
        own[q][i] = e;
      }
      feed(N, IC<18>{}, IC<2>{}, IC<2>{}, IC<2>{});
      if constexpr (n >= 34 && n < 50 && (n & 3) == 2) { u32 e = (l16 & 48) ? wq[(n - 34) / 4] : ONE2; PIN(e); wq[(n - 34) / 4] = e; }
      gather(N, IC<50>{}, IC<1>{});
    });
    half(IC<4>{}, IC<12>{}, [&](auto N) { exch(IC<4>{}, N, IC<0>{}); feed(N, IC<18>{}, IC<3>{}, IC<0>{}, IC<2>{}); gather(N, IC<50>{}, IC<2>{}); });
    half(IC<5>{}, IC<11>{}, [&](auto N) {
      exch(IC<5>{}, N, IC<9>{});
      feed(N, IC<18>{}, IC<3>{}, IC<2>{}, IC<2>{});
      if constexpr (N.v == 48) PIN(own[0], own[1], own[2], own[3], wq);
    });
    half(IC<6>{}, IC<63>{}, [&](auto N) { exch(IC<6>{}, N, IC<0>{}); });
    half(IC<7>{}, IC<0>{}, [&](auto N) { exch(IC<7>{}, N, IC<0>{}); if constexpr (N.v == 56) fresh(); });
    STAMP(31);
    VMW<3>();
    finish(x1p, W_XF1, W_STAT1);
    SETM0(gm0);
    asm volatile("s_nop 0");
    dma1<3072>(gvo, rG, 3 * (64 * 3584 - 1024));
  }
  v4u rec2, crec;
  int xso[4];
  {
    int fo = W_F2 + (p2 * 16 + ec) * 128, so = W_STAT1 + (2 * p1 + (w >> 1)) * 8192 + (w & 1) * 256, so2 = so + 4096, po = W_F2 + (p1 >> 1) * 2048;
    int svo = g * 512 + c * 16, c128 = c * 128, slot = 64 * p1 + 16 * w + c, stw = L_ST + w * 128 + c * 4;
    pvo = (w == 1 && g == 0) ? c * 8 : OOB;
#pragma unroll
    for (int r = 0; r < 4; ++r) { xso[r] = W_XF1 + (4 * p1 + w) * 16384 + ((r + p1) & 3) * 4096; PINS(xso[r]); }
    PINS(fo, so, so2, po); PIN(pvo, svo, c128, slot, stw);
    VMBAR<1>();
    st8c(ft, ws, pvo, fo);
    STAMP(8);
    v4u sv[4];
    ald16c(sv[0], ws, svo, so); ald16c<2048>(sv[1], ws, svo, so); ald16c(sv[2], ws, svo, so2); ald16c<2048>(sv[3], ws, svo, so2);
    DRAIN();
    PIN(sv[0], sv[1], sv[2], sv[3]);
    settle<4>(sv, tag, [&](int i) { return ld16c(ws, svo + i * 2048, so); });
    if (w == 0) ald8c(fl, ws, c128, po);
    float s_ = (asf(sv[0].x) + asf(sv[1].x)) + (asf(sv[2].x) + asf(sv[3].x)), d_ = (asf(sv[0].y) + asf(sv[1].y)) + (asf(sv[2].y) + asf(sv[3].y));
    float q_ = (asf(sv[0].z) + asf(sv[1].z)) + (asf(sv[2].z) + asf(sv[3].z));
    s_ = sum32(sum16(s_)); q_ = sum32(sum16(q_)); d_ = sum32(sum16(d_));
    const float mean = s_ * (1.f / 512), var = fmaxf(q_ * (1.f / 512) - mean * mean, 0.f), rs = __builtin_amdgcn_rsqf(var + EPS);
    const float c3 = (1.f + rs) * __builtin_amdgcn_rsqf(var * (1.f + rs) * (1.f + rs) + EPS);
    *(LAS float*)(L + stw) = mean; *(LAS float*)(L + stw + 64) = c3 * sgn;
    rec2 = v4u{asu(mean), asu(rs), asu(c3), tag};
    crec.x = mkkey((d_ - mean * Wsum) * rs, slot);
    PIN(rec2, crec);
    if (w == 0) { DRAIN(); PIN(fl); check_flags(fl, ft, ws, c128, po); }
  }
  BAR();
  STAMP(9);
  v4u cd[2], tk[2];
  int cdo;
  {
    dma4c(L_HI + w4k, l16, ws, xso[0]);
    dma4c(L_HI + 16384 + w4k, l16, ws, xso[1]);
    dma4c(L_HI + 32768 + w4k, l16, ws, xso[2]);
    hi_a = L_HI + l16; sta = L_ST + g * 16;
    PIN(hi_a, sta);
    sfor<0, 4>([&](auto I) { mn[I.v] = *(const LAS v4f*)(L + sta + I.v * 128); });
    u32 hi = crec.x, lo = 0u;
    top2_row(hi, lo);
    crec = v4u{hi, lo, tag, tag};
    sfor<0, 4>([&](auto I) { acc[I.v * 2] = mn[I.v] * -RS[0]; acc[I.v * 2 + 1] = mn[I.v] * -RS[1]; PIN(acc[I.v * 2], acc[I.v * 2 + 1]); });
    const bool pub2 = i1 == (p1 & 7);
    int rvo = pub2 && g == 0 ? l16 : OOB, cvo = pub2 && lane < 8 ? lane * 4096 : OOB, c16 = c * 16;
    int ro = W_TOK2 + (p1 * 4 + w) * 256, co = W_CAND2 + p1 * 128 + w * 16, ci = W_CAND2 + xcd * 4096, to = W_TOK2 + tb2 * 512;
    cdo = (lane >> 2) * 128 + (lane & 3) * 16;
    abo += W_ABF2;
    PIN(rvo, cvo, crec, cdo, c16); PINS(ro, co, ci, to, abo);
    auto rdx = [&](auto F) { constexpr int f = F.v; return *(const LAS v4u*)(L + hi_a + (f >> 4) * 16384 + (f & 3) * 4096 + ((f >> 2) & 3) * 1024); };
    VMBAR<8>();
    sfor<0, 8>([&](auto F) { R[F.v] = rdx(F); });
    dma4c(L_HI + 49152 + w4k, l16, ws, xso[3]);
    sfor<0, 4>([&](auto Rr) {
      constexpr int r = Rr.v;
      pass<16, (r < 3)>(R, [&](auto F) { return rdx(IC<16 * r + F.v>{}); }, [&](auto F) { return rdx(IC<(16 * r + 16 + F.v) & 63>{}); },
        [&](auto F, auto H, const v4u& fr) { constexpr int f = 16 * r + F.v; MFMA(acc[(f & 3) * 2 + H.v], fr, dfr[(f >> 2) * 2 + H.v]); },
        [&]() { VMBAR<(r == 0 ? 10 : r == 1 ? 6 : 2)>(); },
        [&](auto N) {
          constexpr int n = N.v;
          if constexpr (r == 0 && n == 4) st16c(crec, ws, cvo, co);
          if constexpr (r == 0 && n == 8) st16c(rec2, ws, rvo, ro);
          if constexpr (r == 3) {
            if constexpr (n == 0) ald16c(cd[0], ws, cdo, ci);
            if constexpr (n == 4) ald16c<2048>(cd[1], ws, cdo, ci);
            if constexpr (n == 8) ald16c(tk[0], ws, c16, to);
            if constexpr (n == 12) ald16c<256>(tk[1], ws, c16, to);
            if constexpr (n >= 16 && n < 20) sc[n - 16] = *(const LAS v4f*)(L + sta + 64 + (n - 16) * 128);
          }
        });
    });
    STAMP(10);
    NOPS<7>(acc[7]);
    gemm1_out(IC<1>{}, abo);
  }

  u32 key1, key2;
  v4u xrow[4], wf[4], trec;
  v2f wdf[8], cs2;
  float gate;
  int hpw, hpr, tid16;
  auto request_rows = [&](int xf, int tok) {
    const u32 kr = (lane & 8) ? key2 : key1;
    const int vo = c < 2 ? slot_xf(c ? key2 : key1) + g * 256 : OOB;
    sfor<0, 4>([&](auto K) { ald16c<K.v * 1024>(xrow[K.v], ws, vo, xf + w4k); });
    ald16c(trec, ws, (kr & 2047u) * 16, tok);
    gate = sigm(key_r(kr));
    PIN(gate);
  };
  auto gate_up = [&]() {
    v4f h;
    PIN(xrow[0], xrow[1], xrow[2], xrow[3], trec);
    sfor<0, 4>([&](auto K) { if constexpr (K.v == 0) MFMA0(h, wf[0], xrow[0]); else MFMA(h, wf[K.v], xrow[K.v]); });
    NOPS<7>(h);
    if (c < 2) *(LAS v4f*)(L + hpw) = h;
  };
  auto sum_hp = [&](int a) {
    v2f t[4];
#pragma unroll
    for (int i = 0; i < 4; ++i) t[i] = v2f{*(const LAS float*)(L + a + i * 192), *(const LAS float*)(L + a + i * 192 + 32)};
    return (t[0] + t[1]) + (t[2] + t[3]);
  };
  auto swiglu = [&](int tok) {
    v2f t = sum_hp(hpr);
    settle<1>(&trec, tag, [&](int) { return ld16c(ws, (((lane & 8) ? key2 : key1) & 2047u) * 16, tok); });
    t = (t - asf(trec.x) * cs2) * asf(trec.y);
    return t.x * sigm(t.x) * (t.y * gate);
  };
  auto down_share = [&](float h, int so) {
    v2f y0 = {0.f, 0.f}, y1 = {0.f, 0.f};
    sfor<0, 8>([&](auto Qq) { y0 += dppf<0x150 + Qq.v>(h) * wdf[Qq.v]; y1 += dppf<0x158 + Qq.v>(h) * wdf[Qq.v]; PIN(y0, y1); });
    st16c(v4u{pk(y0.x, y0.y), pk(y1.x, y1.y), tag, tag}, ws, tid16, so);
  };
  v4u pyv[7], sel;
  int pyo, pxw;
  auto consts_py = [&]() {
    const u32 one = 0x3f80u << (16 * (c & 1));
    sel = v4u{(c & 2) ? 0u : one, (c & 2) ? one : 0u, 0u, 0u};
    pyo = isGU ? OOB : (w * 8 + (lane >> 3)) * 4096 + (lane & 7) * 16 + rho * 128;
    pxw = L_X + w * 128 + (lane & 7) * 16;
    PIN(sel, pyo, pxw);
  };

  fresh();
  u32 cc[4], xab;
  float h2;
  {
    int trA = ((c >> 1) & 1 ? L_WU : L_WG) + (4 * g + (c >> 2)) * 16 + (c & 1) * 8 + w * 2048, fo = W_F3 + (p1 * 8 + i1) * 128;
    tid16 = w * 1024 + l16;
    int a = L_WD + (tid16 >> 2);
    PIN(trA, a);
    sfor<0, 4>([&](auto K) {
      const v2u lo = __builtin_bit_cast(v2u, __builtin_amdgcn_ds_read_tr16_b64_v4i16((LAS v4s*)(L + trA + K.v * 512)));
      const v2u hi = __builtin_bit_cast(v2u, __builtin_amdgcn_ds_read_tr16_b64_v4i16((LAS v4s*)(L + trA + K.v * 512 + 256)));
      wf[K.v] = v4u{lo.x, lo.y, hi.x, hi.y};
    });
    u32 wd[8];
#pragma unroll
    for (int i = 0; i < 8; ++i) wd[i] = *(const LAS u32*)(L + a + i * 1024);
    v4f cs;
    ones = v4u{ONE2, ONE2, ONE2, ONE2};
    pvo = (w == 1 && g == 0) ? c * 8 : OOB;
    hpw = L_HP + w * 192 + c * 64 + g * 16; hpr = L_HP + (lane & 8) * 8 + (lane & 7) * 4;
    PIN(ones, pvo, tid16, hpw, hpr); PINS(fo);
    PIN(wf[3], wf[2], wf[1]); NOPS<1>(wf[0]);
    sfor<0, 4>([&](auto K) { if constexpr (K.v == 0) MFMA0(cs, wf[0], ones); else MFMA(cs, wf[K.v], ones); });
#pragma unroll
    for (int i = 0; i < 8; ++i) { wdf[i] = v2f{lo16(wd[i]), hi16(wd[i])}; PIN(wdf[i]); }
    PIN(cs);
    if (c == 2) *(LAS v4f*)(L + hpw) = cs;
    sfor<0, 4>([&](auto I) { tbase[I.v] += W_ABF2 - W_ABF1; PINS(tbase[I.v]); });
    sfor<0, 3>([&](auto I) { xout[I.v] += W_EXC2 - W_EXC1; PINS(xout[I.v]); });
    xin += W_EXC2 - W_EXC1;
    PINS(xin);
    consts2();
    VMBAR<0>();
    st8c(ft, ws, pvo, fo);
    STAMP(11);
    PIN(cd[0], cd[1], tk[0], tk[1]);
    settle<2>(cd, tag, [&](int i) { return ld16c(ws, cdo + i * 2048, W_CAND2 + xcd * 4096); });
    key1 = umax(cd[0].x, cd[1].x); key2 = umax(umin(cd[0].x, cd[1].x), umax(cd[0].y, cd[1].y));
    top2_row(key1, key2); top2_16(key1, key2); top2_32(key1, key2);
    request_rows(W_XF1, W_TOK2);
    cs2 = sum_hp((hpr & ~64) + 128);
    settle<2>(tk, tag, [&](int q) { return ld16c(ws, c * 16 + q * 256, W_TOK2 + tb2 * 512); });
    sfor<0, 4>([&](auto Qq) {
      constexpr int q = Qq.v;
      const float r1 = 1.f + asf(tk[q >> 1].y), mr = -asf(tk[q >> 1].x) * asf(tk[q >> 1].y);
      const u32 a0 = x1p[q >> 1][(q & 1) * 2], a1 = x1p[q >> 1][(q & 1) * 2 + 1];
      own[q] = v4f{lo16(a0), hi16(a0), lo16(a1), hi16(a1)} * r1 + mr;
      PIN(own[q]);
    });
    PIN(cs2);
    ald8c(fl, ws, fvo, W_F3);
    int rwa = L_RW + w * 1280 + 640 + c * 320 + g * 16;
    PIN(rwa);
    VMW<1>();
    gate_up();
    if (c < 2) sfor<0, 4>([&](auto K) { *(LAS v4u*)(L + rwa + K.v * 64) = xrow[K.v]; });
    VMBAR<0>();
    STAMP(12);
    PIN(fl);
    check_flags(fl, ft, ws, fvo, W_F3);
    STAMP(15);
  }
  phase = 4;
  v4u x3p[2];
  {
    dma4c(img(0, 0), l16, ws, tbase[0] + qoff[0]);
    dma4c(img(0, 1), l16, ws, tbase[0] + qoff[1]);
    h2 = swiglu(W_TOK2);
    down_share(h2, W_PY2 + cg * 4096);
    dma4c(img(0, 2), l16, ws, tbase[0] + qoff[2]);
    int t20 = slot_tok(rfl((int)key1) & 2047), t21 = slot_tok(rfl((int)key2) & 2047);
    const rsrc_t rCr = mk(P.Cr, 8 * MB), rCi = mk(P.Ci, 8 * MB);
    const int nn = 4 * b + w;
    const rsrc_t rDn = mk((const char*)(nn < 512 ? P.Dr : P.Di) + (nn & 511) * 1024, 1024);
    int cvo = w == 2 ? (8 * b + (lane & 7)) * 4096 : OOB;
    PINS(t20, t21); PIN(cvo);
    asm volatile("" ::"s"(rCr), "s"(rCi), "s"(rDn));
    VMBAR<9>();
    sfor<0, 8>([&](auto F) { R[F.v] = rdt(IC<0>{}, F); });
    dma4c(img(0, 3), l16, ws, tbase[0] + qoff[3]);
    v4u cb[2], ga[2], fr4[4], gu[2];
    v4f t4, G, rd4[4];
    float e0, e1, km, kr, kc, kv, sm4[4];
    u32 bad, tz, u0, u1;
    int avo, bvo, uvo, gra, grw, grr, pxr, gvo2, uwa;
    sfor<0, 2>([&](auto Rr) {
      constexpr int r = Rr.v;
      pass<16, true>(R, [&](auto F) { return rdt(IC<0>{}, IC<16 * r + F.v>{}); }, [&](auto F) { return rdt(IC<0>{}, IC<16 * r + 16 + F.v>{}); },
        [&](auto F, auto H, const v4u& fr) { mm2(IC<0>{}, IC<16 * r + F.v>{}, H, fr); },
        [&]() { VMBAR<(r == 0 ? 13 : 8)>(); },
        [&](auto N) {
          constexpr int n = N.v;
          feed(N, IC<4>{}, IC<1>{}, Rr, IC<1>{});
          if constexpr (r == 0 && n == 20) consts_py();
          if constexpr (r == 1 && n >= 24 && n < 32 && (n & 1) == 0) ald2(cc[(n - 24) / 2], (n & 4) ? rCi : rCr, cvo, ((n & 2) ? t21 : t20) * 2);
        });
    });
    half(IC<1>{}, IC<21>{}, [&](auto N) {
      constexpr int n = N.v;
      feed(N, IC<0>{}, IC<1>{}, IC<2>{}, IC<2>{});
      if constexpr (n == 32) SETM0(L_DR + w * 1088);
      if constexpr (n == 34) dma1<0>(l16, rDn, 0);
      if constexpr (n == 36) ald16(wq, rW, g * 16, ec * 64);
      if constexpr (n >= 40 && n < 54 && (n & 1) == 0) ald16c(pyv[(n - 40) / 2], ws, pyo, W_PY2 + (n - 40) / 2 * 131072);
      if constexpr (n == 1) { e0 = dppf<0x150>(asf(trec.x)); PIN(e0); }
      if constexpr (n == 3) { e1 = dppf<0x158>(asf(trec.x)); PIN(e1); }
      if constexpr (n == 4) { km = (l16 & 16) ? e1 : e0; PIN(km); }
      if constexpr (n == 5) { e0 = dppf<0x150>(asf(trec.y)); PIN(e0); }
      if constexpr (n == 7) { e1 = dppf<0x158>(asf(trec.y)); PIN(e1); }
      if constexpr (n == 8) { kr = (l16 & 16) ? e1 : e0; PIN(kr); }
      if constexpr (n == 9) { e0 = dppf<0x150>(asf(trec.z)); PIN(e0); }
      if constexpr (n == 11) { e1 = dppf<0x158>(asf(trec.z)); PIN(e1); }
      if constexpr (n == 12) { kc = (l16 & 16) ? e1 : e0; PIN(kc); }
      if constexpr (n == 13) { kr += 1.f; PIN(kr); }
      if constexpr (n == 17) { kv = __builtin_amdgcn_rcpf(kc); PIN(kv); }
      if constexpr (n == 20) { kv *= kr; PIN(kv); }
      if constexpr (n == 21) { kv *= kv; PIN(kv); }
      if constexpr (n == 24) { pxr = L_X + (lane & 7) * 16; PIN(pxr); }
      if constexpr (n == 28) { uvo = !isGU && w == 0 && lane < 16 ? (lane & 7) * 32 + (lane & 8) * 2 : OOB; PIN(uvo); }
    });
    half(IC<2>{}, IC<11>{}, [&](auto N) {
      constexpr int n = N.v;
      if constexpr (n == 0) { VMW<17>(); PIN(cc[0], cc[1], cc[2], cc[3]); u0 = cc[0] | (cc[1] << 16); PIN(u0); }
      if constexpr (n == 1) { u1 = cc[2] | (cc[3] << 16); PIN(u1); }
      if constexpr (n == 2) { u0 = (lane & 8) ? u1 : u0; PIN(u0); }
      if constexpr (n == 4) { const int t = 8 * b + (lane & 7); u1 = (t == t20 ? 0x3f80u : 0u) | (t == t21 ? 0x3f800000u : 0u); PIN(u1); }
      exch(IC<2>{}, N, IC<0>{});
      if constexpr (n == 11) { u0 = (lane & 16) ? u1 : u0; PIN(u0); }
      if constexpr (n == 13) { cvo = w == 2 && lane < 24 ? (lane >> 3) * 32768 + (lane & 7) * 16 : OOB; PIN(cvo); }
      if constexpr (n == 18) st16c(v4u{u0, 0u, tag, 0u}, ws, cvo, W_CB + b * 128);
      feed(N, IC<20>{}, IC<2>{}, IC<0>{}, IC<2>{});
      if constexpr (n == 36) {
        VMW<7>();
        sfor<0, 7>([&](auto I) { PIN(pyv[I.v]); });
        PIN(wq);
        bad = pyv[0].w ^ tag; PIN(bad);
      }
      if constexpr (n == 37 || n == 39 || n == 40) { constexpr int i = n == 37 ? 1 : n == 39 ? 3 : 5; bad = differ(bad, pyv[i].w, tag); bad = differ(bad, pyv[i + 1].w, tag); PIN(bad); }
      if constexpr (n == 41) { bad = isGU ? 0u : bad; PIN(bad); }
      if constexpr (n == 43) {
        resettle<7>(bad, pyv, tag, [&](int i) { return ld16c(ws, pyo, W_PY2 + i * 131072); });
        sfor<0, 7>([&](auto I) { if constexpr (I.v == 0) MFMA0(t4, sel, pyv[0]); else MFMA(t4, sel, pyv[I.v]); });
      }
      if constexpr (n == 49) PIN(t4);
      if constexpr (n == 49 || n == 51) { constexpr int i = n - 49; float a = t4[i] + dppf<0x128>(t4[i]), bb = t4[i + 1] + dppf<0x128>(t4[i + 1]); PIN(a, bb); G[i] = a; G[i + 1] = bb; }
      if constexpr (n == 52) *(LAS v4f*)(L + pxw) = G;
      if constexpr (n == 60 || n == 62) { rd4[n - 60] = *(const LAS v4f*)(L + pxr + (n - 60) * 128); rd4[n - 59] = *(const LAS v4f*)(L + pxr + (n - 59) * 128); }
    });
    half(IC<3>{}, IC<9>{}, [&](auto N) {
      constexpr int n = N.v;
      if constexpr (n >= 4 && n < 8) { constexpr int i = n - 4; float a = rd4[0][i] + rd4[1][i], bb = rd4[2][i] + rd4[3][i]; PIN(a, bb); rd4[0][i] = a; rd4[2][i] = bb; }
      if constexpr (n == 8 || n == 9) { constexpr int i = n - 8; float a = rd4[0][i] + rd4[2][i], bb = rd4[0][i + 2] + rd4[2][i + 2]; PIN(a, bb); sm4[2 * i] = a; sm4[2 * i + 1] = bb; }
      if constexpr (n == 10) { CVT(u0, sm4[0], sm4[1]); CVT(u1, sm4[2], sm4[3]); }
      if constexpr (n == 12) st16c(v4u{(lane & 8) ? u1 : u0, 0u, 0u, tag}, ws, uvo, W_G + 16384 + rho * 256);
      feed(N, IC<16>{}, IC<2>{}, IC<2>{}, IC<2>{});
      if constexpr (n >= 20 && n < 36 && (n & 3) == 0) { u32 e = (l16 & 48) ? wq[(n - 20) / 4] : ONE2; PIN(e); wq[(n - 20) / 4] = e; }
      if constexpr (n == 36) { u32 o = ONE2; PIN(o); *(LAS u32*)(L + L_ON + lane * 4) = o; }
      if constexpr (n == 40) { gra = (c < 4 ? L_RW + w * 1280 + c * 320 : c < 8 ? L_ON : L_DR + (c & 3) * 1088 + w * 256) + g * 16; PIN(gra); }
      if constexpr (n == 44) { grw = L_GR + w * 1024 + l16; grr = L_GR + c * 16; PIN(grw, grr); }
      if constexpr (n == 48) { uwa = L_RW + w * 1280 + lane * 2; PIN(uwa); }
      if constexpr (n == 50) { gvo2 = w == 3 && (lane >> 2) == 2 ? (lane & 3) * 16 : OOB; PIN(gvo2); }
    });
    half(IC<4>{}, IC<12>{}, [&](auto N) {
      constexpr int n = N.v;
      exch(IC<4>{}, N, IC<0>{});
      if constexpr (n == 18) ald16c(gu[0], ws, l16, W_G + 16384 + w * 2048);
      if constexpr (n == 19) ald16c<1024>(gu[1], ws, l16, W_G + 16384 + w * 2048);
      feed(N, IC<20>{}, IC<3>{}, IC<0>{}, IC<2>{});
      if constexpr (n == 25) { avo = ((g < 3 ? g : 0) * 512 + 32 * ec + 8 * (c >> 2) + (c & 3)) * 16; PIN(avo); }
      if constexpr (n == 33) { bvo = g < 3 ? g * 32768 + (32 * tb2 + 8 * (c >> 2) + (c & 3)) * 16 : OOB; tz = g < 3 ? tag : 0u; PIN(bvo, tz); }
    });
    half(IC<5>{}, IC<10>{}, [&](auto N) {
      constexpr int n = N.v;
      if constexpr (n == 0) { VMW<8>(); PIN(gu[0], gu[1]); settle<2>(gu, tag, [&](int q) { return ld16c(ws, l16 + q * 1024, W_G + 16384 + w * 2048); }); }
      if constexpr (n == 2 || n == 4) *(LAS u16*)(L + uwa + (n - 2) * 64) = (u16)gu[(n - 2) / 2].x;
      if constexpr (n == 3 || n == 5) *(LAS u16*)(L + uwa + 320 + (n - 3) * 64) = (u16)(gu[(n - 3) / 2].x >> 16);
      exch(IC<5>{}, N, IC<8>{});
      if constexpr (n == 6 || n == 10) { CLOB(); fr4[(n - 6) / 2] = *(const LAS v4u*)(L + gra + (n - 6) * 32); fr4[(n - 6) / 2 + 1] = *(const LAS v4u*)(L + gra + (n - 6) * 32 + 64); }
      if constexpr (n == 19) { PIN(fr4[3], fr4[2], fr4[1], fr4[0]); MFMA0(G, fr4[0], fr4[0]); MFMA(G, fr4[1], fr4[1]); MFMA(G, fr4[2], fr4[2]); MFMA(G, fr4[3], fr4[3]); }
      feed(N, IC<12>{}, IC<3>{}, IC<2>{}, IC<2>{});
      if constexpr (n == 25) { PIN(G); *(LAS v4f*)(L + grw) = G; }
      if constexpr (n == 31) asm volatile("s_waitcnt lgkmcnt(3)\n\ts_barrier" ::: "memory");
      if constexpr (n == 32 || n == 33) { rd4[2 * n - 64] = *(const LAS v4f*)(L + grr + (2 * n - 64) * 1024); rd4[2 * n - 63] = *(const LAS v4f*)(L + grr + (2 * n - 63) * 1024); }
      if constexpr (n == 35 || n == 36) { sm4[2 * n - 70] = *(const LAS float*)(L + grr + (2 * n - 70) * 1024 + 256); sm4[2 * n - 69] = *(const LAS float*)(L + grr + (2 * n - 69) * 1024 + 256); }
      if constexpr (n >= 43 && n < 47) { constexpr int i = n - 43; float a = rd4[0][i] + rd4[1][i], bb = rd4[2][i] + rd4[3][i]; PIN(a, bb); rd4[0][i] = a; rd4[2][i] = bb; }
      if constexpr (n == 47) { sm4[0] += sm4[1]; PIN(sm4[0]); sm4[2] += sm4[3]; PIN(sm4[2]); }
      if constexpr (n == 48 || n == 49) { constexpr int i = 2 * n - 96; float a = rd4[0][i] + rd4[2][i], bb = rd4[0][i + 1] + rd4[2][i + 1]; PIN(a, bb); t4[i] = a; t4[i + 1] = bb; }
      if constexpr (n == 50) { sm4[0] += sm4[2]; PIN(sm4[0]); e0 = (l16 & 16) ? t4[1] : t4[0]; PIN(e0); }
      if constexpr (n == 51) { e1 = (l16 & 16) ? t4[3] : t4[2]; PIN(e1); sm4[1] = sm4[0] * (1.f / 512); PIN(sm4[1]); }
      if constexpr (n == 52) { e0 -= sm4[1] * sm4[0]; PIN(e0); e1 -= km * sm4[0]; PIN(e1); }
      if constexpr (n == 53) { e1 *= kr; PIN(e1); e0 = e0 * (1.f / 512) + kv; PIN(e0); }
      if constexpr (n == 60) { e0 += e1 * (1.f / 256); PIN(e0); e0 = fmaxf(e0, EPS); PIN(e0); }
      if constexpr (n == 62) { e0 = __builtin_amdgcn_rsqf(e0); PIN(e0); }
    });
    half(IC<6>{}, IC<63>{}, [&](auto N) {
      constexpr int n = N.v;
      if constexpr (n == 0) { e1 = e0 * kr - kc; PIN(e1); sm4[1] *= e0; PIN(sm4[1]); }
      if constexpr (n == 1) { sm4[1] += e1 * km; PIN(sm4[1]); }
      if constexpr (n == 2) { sm4[2] = dppf<0x150>(e0) * t4[0]; PIN(sm4[2]); sm4[3] = dppf<0x151>(e0) * t4[1]; PIN(sm4[3]); }
      if constexpr (n == 4) { sm4[2] += dppf<0x150>(e1) * t4[2]; PIN(sm4[2]); sm4[3] += dppf<0x151>(e1) * t4[3]; PIN(sm4[3]); }
      if constexpr (n == 5) { sm4[0] = -sm4[0]; PIN(sm4[0]); }
      if constexpr (n == 7) { sm4[2] += dppf<0x150>(sm4[1]) * sm4[0]; PIN(sm4[2]); }
      if constexpr (n == 9) { sm4[3] += dppf<0x151>(sm4[1]) * sm4[0]; PIN(sm4[3]); }
      if constexpr (n == 11) { u0 = pk(sm4[2], sm4[3]) ^ (b < 128 ? 0u : 0x80008000u); PIN(u0); st16c(v4u{u0, 0u, 0u, tag}, ws, gvo2, W_G + b * 64); }
      exch(IC<6>{}, N, IC<0>{});
    });
    half(IC<7>{}, IC<0>{}, [&](auto N) {
      constexpr int n = N.v;
      if constexpr (n == 2) ald16c(cb[0], ws, bvo, W_CB);
      if constexpr (n == 4) ald16c<64>(cb[1], ws, bvo, W_CB);
      exch(IC<7>{}, N, IC<0>{});
      if constexpr (n == GA0) ald16c(ga[0], ws, avo, W_G);
      if constexpr (n == GA0 + 2) ald16c<64>(ga[1], ws, avo, W_G);
      if constexpr (n == 56) fresh();
    });
    STAMP(29);
    VMW<3>();
    PIN(ga[0], ga[1], cb[0], cb[1]);
    settle<2>(ga, tag, [&](int q) { return ld16c(ws, avo + q * 64, W_G); });
    settle<2, 2>(cb, tz, [&](int q) { return ld16c(ws, bvo + q * 64, W_CB); });
    sfor<0, 4>([&](auto Qq) { MFMA(own[Qq.v], ga[Qq.v & 1], cb[Qq.v >> 1]); });
    finish(x3p, W_XF3, W_STAT3);
  }
  {
    int fo = W_F4 + (p2 * 16 + ec) * 128, so = W_STAT3 + tb2 * 8192, so2 = so + 4096;
    pvo = (w == 1 && g == 0) ? c * 8 : OOB;
    cdo = (lane >> 2) * 128 + (lane & 3) * 16;
    PINS(fo, so, so2); PIN(pvo, cdo);
    VMBAR<0>();
    st8c(ft, ws, pvo, fo);
    STAMP(17);
    if (ec == p2) {
      v4u v[8];
      sfor<0, 8>([&](auto I) { ald16c<(I.v & 3) * 1024>(v[I.v], ws, l16, I.v < 4 ? so : so2); });
      int c128 = c * 128, slot = 32 * tb2 + (lane & 31), tvo = lane < 32 ? l16 : OOB, cvo = lane < 8 ? lane * 2048 : OOB;
      int po = W_F4 + p2 * 2048, to = W_TOK4 + tb2 * 512, co = W_CAND4 + p2 * 128 + w * 16;
      PIN(c128, slot, tvo, cvo); PINS(po, to, co);
      DRAIN();
      sfor<0, 8>([&](auto I) { PIN(v[I.v]); });
      settle<8>(v, tag, [&](int i) { return ld16c(ws, l16 + i * 1024, so); });
      ald8c(fl, ws, c128, po);
      float s_ = 0.f, q_ = 0.f, d_ = 0.f;
#pragma unroll
      for (int i = 0; i < 8; i += 2) { s_ += asf(v[i].x) + asf(v[i + 1].x); d_ += asf(v[i].y) + asf(v[i + 1].y); q_ += asf(v[i].z) + asf(v[i + 1].z); }
      s_ = sum32(s_); q_ = sum32(q_); d_ = sum32(d_);
      const float mean = s_ * (1.f / 512), rs = __builtin_amdgcn_rsqf(fmaxf(q_ * (1.f / 512) - mean * mean, 0.f) + EPS);
      st16c(v4u{asu(mean), asu(rs), 0u, tag}, ws, tvo, to);
      u32 hi = mkkey((d_ - mean * Wsum) * rs, slot), lo = 0u;
      top2_row(hi, lo); top2_16(hi, lo);
      crec = v4u{hi, lo, tag, tag};
      PIN(crec);
      VMW<1>();
      PIN(fl);
      check_flags(fl, ft, ws, c128, po);
      st16c(crec, ws, cvo, co);
      if (w == 0) DRAIN();
    } else if (w == 0) {
      for (int z = 0; z < CSLEEP; ++z) __builtin_amdgcn_s_sleep(4);
    }
  }
  STAMP(18);
  if (w == 0) {
    v4u a;
    ald16c(a, ws, cdo, W_CAND4 + xcd * 2048);
    DRAIN();
    PIN(a);
    settle<1>(&a, tag, [&](int) { return ld16c(ws, cdo, W_CAND4 + xcd * 2048); });
    STAMP(30);
    key1 = a.x; key2 = a.y;
    top2_row(key1, key2); top2_16(key1, key2); top2_32(key1, key2);
    *(LAS v2u*)(L + L_H) = v2u{key1, key2};
  }
  BAR();
  { const v2u k = *(const LAS v2u*)(L + L_H); key1 = k.x; key2 = k.y; }
  request_rows(W_XF3, W_TOK4);
  ald16c(tk[0], ws, c * 16, W_TOK4 + tb2 * 512); ald16c<256>(tk[1], ws, c * 16, W_TOK4 + tb2 * 512);
  ald4c(xab, ws, isGU ? OOB : slot_xf((lane & 8) ? key2 : key1) + ((lane >> 2) & 1) * 256 + (lane & 3) * 4, W_XF3 + (rho >> 1) * 1024 + (rho & 1) * 512);
  int pyso = W_PY4 + cg * 4096;
  PINS(pyso);
  VMW<3>();
  gate_up();
  BAR();
  down_share(swiglu(W_TOK4), pyso);
  STAMP(19);
  const rsrc_t rO = mk(P.out, 2 * MB);
  VMW<1>();
  PIN(tk[0], tk[1], xab);
  settle<2>(tk, tag, [&](int q) { return ld16c(ws, c * 16 + q * 256, W_TOK4 + tb2 * 512); });
  sfor<0, 2>([&](auto MT) {
    constexpr int mt = MT.v;
    const float r1 = 1.f + asf(tk[mt].y), mr = -asf(tk[mt].x) * asf(tk[mt].y);
    v4u o;
#pragma unroll
    for (int q = 0; q < 4; ++q) o[q] = pk(lo16(x3p[mt][q]) * r1 + mr, hi16(x3p[mt][q]) * r1 + mr);
    const u32 slot = 32 * tb2 + 16 * mt + c;
    st16(o, rO, slot != (key1 & 2047u) && slot != (key2 & 2047u) ? (32 * tb2 + 8 * (c >> 2) + 4 * mt + (c & 3)) * 1024 + g * 16 : OOB, ec * 64);
  });
  st8c(ft, ws, b == 0 && w == 0 && lane == 0 ? 0 : OOB, W_EPOCH);
  if (!isGU) {
    for (int z = 0; z < RSLEEP; ++z) __builtin_amdgcn_s_sleep(4);
    sfor<0, 7>([&](auto I) { ald16c(pyv[I.v], ws, pyo, W_PY4 + I.v * 131072); });
    const float r1 = 1.f + asf(trec.y), mr = -asf(trec.x) * asf(trec.y);
    float e0 = lo16(xab) * r1 + mr, e1 = hi16(xab) * r1 + mr;
    int ovo = w == 0 && lane < 16 ? slot_tok(((lane & 8) ? key2 : key1) & 2047u) * 1024 + (lane & 7) * 4 : OOB, a = L_X + (lane & 7) * 16 + (lane & 8);
    PIN(e0, e1, ovo, a);
    DRAIN();
    sfor<0, 7>([&](auto I) { PIN(pyv[I.v]); });
    settle<7>(pyv, tag, [&](int i) { return ld16c(ws, pyo, W_PY4 + i * 131072); });
    v4f t;
    sfor<0, 7>([&](auto I) { if constexpr (I.v == 0) MFMA0(t, sel, pyv[0]); else MFMA(t, sel, pyv[I.v]); });
    NOPS<7>(t);
#pragma unroll
    for (int i = 0; i < 4; ++i) t[i] += dppf<0x128>(t[i]);
    *(LAS v4f*)(L + pxw) = t;
    BAR();
    const v2f u = (*(const LAS v2f*)(L + a) + *(const LAS v2f*)(L + a + 128)) + (*(const LAS v2f*)(L + a + 256) + *(const LAS v2f*)(L + a + 384));
    st4(pk(e0 + u.x, e1 + u.y), rO, ovo, rho * 32);
  }
}

extern "C" void kernel_launch(void* const* d_in, const int* in_sizes, int n_in, void* d_out, int out_size, void* d_ws, size_t ws_size,
                              hipStream_t stream) {
  Params p;
  p.x = (const u16*)d_in[0]; p.Cr = (const u16*)d_in[1]; p.Ci = (const u16*)d_in[2]; p.Dr = (const u16*)d_in[3]; p.Di = (const u16*)d_in[4];
  p.wr = (const u16*)d_in[6]; p.wg = (const u16*)d_in[7]; p.wu = (const u16*)d_in[8]; p.wd = (const u16*)d_in[9];
  p.out = (u16*)d_out; p.ws = (char*)d_ws;
  (void)hipFuncSetAttribute((const void*)fnet_mod_kernel, hipFuncAttributeMaxDynamicSharedMemorySize, L_BYTES);
  fnet_mod_kernel<<<256, 256, L_BYTES, stream>>>(p);
}
```
